```python
import torch

def _softmax(x, axis=-1):
    return torch.softmax(x, dim=axis)
def _gelu(x):
    return torch.nn.functional.gelu(x, approximate="tanh")
def _silu(x):
    return torch.nn.functional.silu(x)
def _sigmoid(x):
    return torch.sigmoid(x)
def _layernorm(x, axis=-1, eps=1e-5):
    m = x.mean(dim=axis, keepdim=True)
    v = x.var(dim=axis, keepdim=True, unbiased=False)
    return (x - m) / torch.sqrt(v + eps)
def _scatter_add(tgt, idx, src, axis=0):
    return tgt.clone().index_add_(axis, idx, src)
def _segment_sum(src, seg, n):
    out = torch.zeros((n,) + src.shape[1:], dtype=src.dtype)
    return out.index_add_(0, seg, src)
def _pad(x, pw):
    flat = tuple(v for pair in reversed(pw) for v in pair)
    return torch.nn.functional.pad(x, flat)

kernel_name = 'standalone__mixer_token_mlp__prompt'

def setup_inputs():

    _w = (lambda a: (lambda t: t.to(torch.bfloat16) if t.is_floating_point() else t)(torch.as_tensor(a)))
    B, S, D, F = 1, 2048, 512, 1792
    return {
        "x": _w((lambda *s: torch.randn(*s, dtype=torch.bfloat16) * 0.05)(B, S, D)),
        "w1": _w((lambda *s: torch.randn(*s, dtype=torch.bfloat16) * 0.2)(S, F) * (S ** -0.5)),
        "b1": _w((lambda *s: torch.randn(*s, dtype=torch.bfloat16) * 3.2)(F) * 0.02),
        "w2": _w((lambda *s: torch.randn(*s, dtype=torch.bfloat16) * 0.05)(F, S) * (F ** -0.5)),
        "b2": _w((lambda *s: torch.randn(*s, dtype=torch.bfloat16) * 0.1)(S) * 0.02),
    }

def reference(x, w1, b1, w2, b2):
    B, S, D = x.shape
    h = _layernorm(x, axis=-1)
    u = torch.einsum("bsd,sf->bdf", h, w1) + b1
    u = _gelu(u)
    y = torch.einsum("bdf,fs->bds", u, w2) + b2
    return (lambda x, *d: x.permute(*d))(y, 0, 2, 1)
```

```cpp
#include <hip/hip_runtime.h>
#include <hip/hip_bf16.h>

typedef unsigned short u16;
using bf16x8 = __attribute__((ext_vector_type(8))) short;
using f32x4  = __attribute__((ext_vector_type(4))) float;

constexpr int S = 2048, D = 512, F = 1792;

__device__ __forceinline__ float bf2f(u16 v) { return __uint_as_float(((unsigned)v) << 16); }
__device__ __forceinline__ u16 f2bf(float f) { __hip_bfloat16 b = __float2bfloat16(f); return *reinterpret_cast<u16*>(&b); }
__device__ __forceinline__ float rbf(float f) { return bf2f(f2bf(f)); }

__global__ __launch_bounds__(256) void ln_kernel(const u16* __restrict__ x, u16* __restrict__ h) {
  int wave = threadIdx.x >> 6, lane = threadIdx.x & 63;
  int s = blockIdx.x * 4 + wave;
  bf16x8 v = *reinterpret_cast<const bf16x8*>(x + (size_t)s * D + lane * 8);
  float f[8]; float sum = 0.f;
  for (int i = 0; i < 8; ++i) { f[i] = bf2f((u16)v[i]); sum += f[i]; }
  for (int o = 32; o > 0; o >>= 1) sum += __shfl_xor(sum, o);
  float mean = sum * (1.0f / D);
  float sq = 0.f;
  for (int i = 0; i < 8; ++i) { float d = f[i] - mean; sq += d * d; }
  for (int o = 32; o > 0; o >>= 1) sq += __shfl_xor(sq, o);
  float var = rbf(sq * (1.0f / D));
  float mb = rbf(mean);
  float den = rbf(sqrtf(rbf(var + 1e-5f)));
  bf16x8 o8;
  for (int i = 0; i < 8; ++i) o8[i] = (short)f2bf(rbf(f[i] - mb) / den);
  *reinterpret_cast<bf16x8*>(h + (size_t)s * D + lane * 8) = o8;
}

template <bool GELU>
__global__ __launch_bounds__(256) void gemm_kernel(const u16* __restrict__ A, const u16* __restrict__ B,
                                                   const u16* __restrict__ bias, u16* __restrict__ C, int M, int K) {
  __shared__ u16 sA[32][64 + 2];
  __shared__ u16 sB[32][64 + 2];
  int m0 = blockIdx.x * 64, n0 = blockIdx.y * 64;
  int wave = threadIdx.x >> 6, lane = threadIdx.x & 63;
  int wm = wave >> 1, wn = wave & 1;
  f32x4 acc[2][2] = {};
  for (int k0 = 0; k0 < K; k0 += 32) {
    for (int e = threadIdx.x; e < 32 * 64; e += 256) {
      int r = e >> 6, c = e & 63;
      sA[r][c] = A[(size_t)(k0 + r) * M + m0 + c];
      sB[r][c] = B[(size_t)(k0 + r) * D + n0 + c];
    }
    __syncthreads();
    for (int i = 0; i < 2; ++i)
      for (int j = 0; j < 2; ++j) {
        bf16x8 a, b;
        for (int q = 0; q < 8; ++q) {
          a[q] = (short)sB[(lane >> 4) * 8 + q][wn * 32 + j * 16 + (lane & 15)];
          b[q] = (short)sA[(lane >> 4) * 8 + q][wm * 32 + i * 16 + (lane & 15)];
        }
        acc[i][j] = __builtin_amdgcn_mfma_f32_16x16x32_bf16(a, b, acc[i][j], 0, 0, 0);
      }
    __syncthreads();
  }
  for (int i = 0; i < 2; ++i)
    for (int j = 0; j < 2; ++j) {
      int m = m0 + wm * 32 + i * 16 + (lane & 15);
      float bv = bf2f(bias[m]);
      for (int q = 0; q < 4; ++q) {
        int n = n0 + wn * 32 + j * 16 + (lane >> 4) * 4 + q;
        float v = rbf(rbf(acc[i][j][q]) + bv);
        if (GELU) {
          float inner = 0.7978845608028654f * (v + 0.044715f * v * v * v);
          v = 0.5f * v * (1.0f + tanhf(inner));
        }
        C[(size_t)m * D + n] = f2bf(v);
      }
    }
}

extern "C" void kernel_launch(void* const* d_in, const int* in_sizes, int n_in,
                              void* d_out, int out_size, void* d_ws, size_t ws_size,
                              hipStream_t stream) {
  const u16* x = (const u16*)d_in[0];
  const u16* w1 = (const u16*)d_in[1];
  const u16* b1 = (const u16*)d_in[2];
  const u16* w2 = (const u16*)d_in[3];
  const u16* b2 = (const u16*)d_in[4];
  u16* out = (u16*)d_out;
  u16* h = (u16*)d_ws;
  u16* u = h + (size_t)S * D;
  ln_kernel<<<S / 4, 256, 0, stream>>>(x, h);
  gemm_kernel<true><<<dim3(F / 64, D / 64), 256, 0, stream>>>(w1, h, b1, u, F, S);
  gemm_kernel<false><<<dim3(S / 64, D / 64), 256, 0, stream>>>(w2, u, b2, out, S, F);
}
```

```cpp
#include <hip/hip_runtime.h>

constexpr int LDS_BYTES = 163840;

__global__ __launch_bounds__(256, 1) void mixer_token_mlp(const void* x, const void* w1, const void* b1, const void* w2,
                                                          const void* b2, void* out, void* ws) {
  extern __shared__ __attribute__((aligned(16))) char lds[];
  const unsigned ldsb = (unsigned)(size_t)lds;
  const void* ka = (const void*)__builtin_amdgcn_kernarg_segment_ptr();
  asm volatile(
      "s_load_dwordx8 s[20:27], %[ka], 0x0\n\t"
      "s_load_dwordx4 s[28:31], %[ka], 0x20\n\t"
      "s_load_dwordx2 s[34:35], %[ka], 0x30\n\t"
      "s_getpc_b64 s[16:17]\n\t"
      "kstart%=:\n\t"
      "s_memrealtime s[96:97]\n\t"
      "s_mov_b32 s57, %[bid]\n\t"
      "v_and_b32 v128, 63, %[tid]\n\t"
      "v_lshrrev_b32 v160, 6, %[tid]\n\t"
      "v_lshlrev_b32 v154, 4, v128\n\t"
      "v_readfirstlane_b32 s56, v160\n\t"
      "v_mov_b32 v129, %[tid]\n\t"
      "s_and_b32 s59, s57, 7\n\t"
      "s_lshr_b32 s58, s57, 3\n\t"
      "v_mov_b32 v153, 0\n\t"
      "s_lshr_b32 s75, s58, 3\n\t"
      "s_and_b32 s63, s58, 7\n\t"
      "s_lshl_b32 s62, s59, 2\n\t"
      "s_add_u32 s62, s62, s75\n\t"
      "s_and_b32 s78, s56, 1\n\t"
      "s_lshr_b32 s79, s56, 1\n\t"
      "s_mul_i32 s70, s56, 28672\n\t"
      "s_add_u32 s70, s70, %[ldsb]\n\t"
      "s_lshr_b32 s72, s59, 1\n\t"
      "s_mul_i32 s72, s72, 7\n\t"
      "s_and_b32 s73, s59, 1\n\t"
      "s_lshl_b32 s74, s73, 2\n\t"
      "s_add_u32 s60, s72, s74\n\t"
      "s_add_u32 s60, s60, s75\n\t"
      "s_add_u32 s72, s72, 3\n\t"
      "s_sub_u32 s73, s58, 24\n\t"
      "s_add_u32 s73, s73, s74\n\t"
      "s_cmp_ge_u32 s58, 24\n\t"
      "s_cselect_b32 s60, s72, s60\n\t"
      "s_cselect_b32 s61, s73, s63\n\t"
      "v_lshrrev_b32 v168, 3, v128\n\t"
      "v_bfe_u32 v169, v128, 4, 1\n\t"
      "v_lshlrev_b32 v169, 2, v169\n\t"
      "v_and_b32 v170, 7, v128\n\t"
      "v_xor_b32 v170, v170, v169\n\t"
      "v_lshlrev_b32 v170, 4, v170\n\t"
      "v_mul_u32_u24 v171, 3584, v168\n\t"
      "v_add_u32 v130, v171, v170\n\t"
      "v_add_u32 v131, 27648, v130\n\t"
      "s_mul_i32 s65, s56, 1835008\n\t"
      "s_lshl_b32 s72, s60, 7\n\t"
      "s_add_u32 s65, s65, s72\n\t"
      "s_add_u32 s66, s65, 1835008\n\t"
      "s_lshl_b32 s72, s61, 2\n\t"
      "s_and_b32 s74, s61, 3\n\t"
      "s_lshl_b32 s74, s74, 3\n\t"
      "s_cmp_ge_u32 s58, 24\n\t"
      "s_cselect_b32 s72, s74, s72\n\t"
      "s_mul_i32 s72, s72, 57344\n\t"
      "s_add_u32 s64, s65, s72\n\t"
      "s_lshl_b32 s72, s57, 13\n\t"
      "s_lshl_b32 s73, s56, 11\n\t"
      "s_add_u32 s72, s72, s73\n\t"
      "s_waitcnt lgkmcnt(0)\n\t"
      "s_add_u32 s18, s20, s72\n\t"
      "s_addc_u32 s19, s21, 0\n\t"
      "global_load_dwordx4 v[160:163], v154, s[18:19]\n\t"
      "global_load_dwordx4 v[164:167], v154, s[18:19] offset:1024\n\t"
      "global_load_dwordx2 v[138:139], v153, s[34:35] sc1\n\t"
      "s_mov_b32 s98, s96\n\t"
      "s_cmp_ge_u32 s58, 28\n\t"
      "s_cbranch_scc1 Lidle%=\n\t"
      "s_mov_b32 s36, s22\n\t"
      "s_and_b32 s37, s23, 0xffff\n\t"
      "s_mov_b32 s38, 7340032\n\t"
      "s_mov_b32 s39, 0x20000\n\t"
      "s_add_u32 m0, s70, 0\n\t"
      "s_add_u32 s99, s64, 57344\n\t"
      "buffer_load_dwordx4 v130, s[36:39], s64 offen lds\n\t"
      "buffer_load_dwordx4 v131, s[36:39], s64 offen offset:1024 lds\n\t"
      "s_cmp_eq_u32 s99, s66\n\t"
      "s_cselect_b32 s64, s65, s99\n\t"
      "s_add_u32 m0, s70, 2048\n\t"
      "s_add_u32 s99, s64, 57344\n\t"
      "buffer_load_dwordx4 v130, s[36:39], s64 offen lds\n\t"
      "buffer_load_dwordx4 v131, s[36:39], s64 offen offset:1024 lds\n\t"
      "s_cmp_eq_u32 s99, s66\n\t"
      "s_cselect_b32 s64, s65, s99\n\t"
      "s_cmp_lg_u32 s56, 3\n\t"
      "s_cbranch_scc1 Lnopf1_%=\n\t"
      "s_cmp_ge_u32 s58, (kend%=-kstart%=)>>10\n\t"
      "s_cbranch_scc1 Lnopf1_%=\n\t"
      "s_lshl_b32 s72, s58, 10\n\t"
      "s_add_u32 s74, s16, s72\n\t"
      "s_addc_u32 s75, s17, 0\n\t"
      "global_load_dwordx4 v[156:159], v154, s[74:75]\n\t"
      "Lnopf1_%=:\n\t"
      "s_lshl_b32 s72, s60, 7\n\t"
      "s_lshl_b32 s73, s79, 6\n\t"
      "s_add_u32 s72, s72, s73\n\t"
      "v_lshrrev_b32 v168, 5, v128\n\t"
      "v_lshlrev_b32 v168, 3, v168\n\t"
      "v_add_u32 v168, s72, v168\n\t"
      "global_load_dwordx2 v[140:141], v168, s[24:25] offset:0\n\t"
      "global_load_dwordx2 v[142:143], v168, s[24:25] offset:16\n\t"
      "global_load_dwordx2 v[144:145], v168, s[24:25] offset:32\n\t"
      "global_load_dwordx2 v[146:147], v168, s[24:25] offset:48\n\t"
      "s_lshl_b32 s72, s62, 6\n\t"
      "s_lshl_b32 s73, s79, 5\n\t"
      "s_add_u32 s72, s72, s73\n\t"
      "v_and_b32 v168, 31, v128\n\t"
      "v_add_u32 v168, s72, v168\n\t"
      "v_lshlrev_b32 v168, 1, v168\n\t"
      "global_load_ushort v148, v168, s[28:29]\n\t"
      "v_lshrrev_b32 v168, 3, v128\n\t"
      "v_lshlrev_b32 v168, 10, v168\n\t"
      "v_bfe_u32 v169, v128, 2, 1\n\t"
      "v_lshl_add_u32 v168, v169, 9, v168\n\t"
      "v_lshlrev_b32 v170, 1, v169\n\t"
      "v_and_b32 v169, 1, v128\n\t"
      "v_lshl_add_u32 v168, v169, 8, v168\n\t"
      "v_add_u32 v170, v170, v169\n\t"
      "v_bfe_u32 v169, v128, 1, 1\n\t"
      "v_lshl_add_u32 v168, v169, 6, v168\n\t"
      "s_lshl_b32 s72, s56, 2\n\t"
      "s_add_u32 s72, s72, %[ldsb]\n\t"
      "s_add_u32 s72, s72, 114688\n\t"
      "v_add_u32 v168, s72, v168\n\t"
      "v_xor_b32 v169, 0, v170\n\t"
      "v_lshl_add_u32 v192, v169, 4, v168\n\t"
      "v_xor_b32 v169, 1, v170\n\t"
      "v_lshl_add_u32 v193, v169, 4, v168\n\t"
      "v_xor_b32 v169, 2, v170\n\t"
      "v_lshl_add_u32 v194, v169, 4, v168\n\t"
      "v_xor_b32 v169, 3, v170\n\t"
      "v_lshl_add_u32 v195, v169, 4, v168\n\t"
      "v_bfe_u32 v169, v129, 4, 2\n\t"
      "v_xor_b32 v169, v129, v169\n\t"
      "v_lshlrev_b32 v169, 4, v169\n\t"
      "s_add_u32 s72, %[ldsb], 114688\n\t"
      "v_add_u32 v196, s72, v169\n\t"
      "v_lshrrev_b32 v169, 5, v128\n\t"
      "v_lshlrev_b32 v169, 10, v169\n\t"
      "v_and_b32 v170, 31, v128\n\t"
      "v_lshl_add_u32 v169, v170, 4, v169\n\t"
      "s_lshl_b32 s72, s56, 18\n\t"
      "v_add_u32 v197, s72, v169\n\t"
      "s_lshr_b32 s76, s57, 1\n\t"
      "s_lshl_b32 s76, s76, 11\n\t"
      "s_and_b32 s77, s57, 1\n\t"
      "s_lshl_b32 s77, s77, 9\n\t"
      "s_add_u32 s76, s76, s77\n\t"
      "s_add_u32 s77, s76, 1048576\n\t"
      "s_lshl_b32 s72, s57, 5\n\t"
      "s_add_u32 s72, s72, 4096\n\t"
      "v_mov_b32 v153, s72\n\t"
      "v_lshrrev_b32 v168, 5, v128\n\t"
      "v_lshlrev_b32 v168, 10, v168\n\t"
      "v_bfe_u32 v169, v128, 2, 2\n\t"
      "v_lshl_add_u32 v168, v169, 7, v168\n\t"
      "v_bfe_u32 v169, v128, 4, 1\n\t"
      "v_lshl_add_u32 v168, v169, 5, v168\n\t"
      "v_and_b32 v169, 3, v128\n\t"
      "v_lshl_add_u32 v168, v169, 3, v168\n\t"
      "v_add_u32 v168, s70, v168\n\t"
      "v_bfe_u32 v169, v128, 3, 1\n\t"
      "v_xor_b32 v169, s79, v169\n\t"
      "v_lshl_add_u32 v134, v169, 6, v168\n\t"
      "v_xor_b32 v169, 1, v169\n\t"
      "v_lshl_add_u32 v135, v169, 6, v168\n\t"
      "s_add_u32 s44, s34, 131072\n\t"
      "s_addc_u32 s45, s35, 0\n\t"
      "s_and_b32 s45, s45, 0xffff\n\t"
      "s_mov_b32 s46, 2097152\n\t"
      "s_mov_b32 s47, 0x20000\n\t"
      "s_waitcnt vmcnt(10)\n\t"
      "s_add_u32 m0, s70, 4096\n\t"
      "s_add_u32 s99, s64, 57344\n\t"
      "buffer_load_dwordx4 v130, s[36:39], s64 offen lds\n\t"
      "buffer_load_dwordx4 v131, s[36:39], s64 offen offset:1024 lds\n\t"
      "s_cmp_eq_u32 s99, s66\n\t"
      "s_cselect_b32 s64, s65, s99\n\t"
      "s_add_u32 m0, s70, 6144\n\t"
      "s_add_u32 s99, s64, 57344\n\t"
      "buffer_load_dwordx4 v130, s[36:39], s64 offen lds\n\t"
      "buffer_load_dwordx4 v131, s[36:39], s64 offen offset:1024 lds\n\t"
      "s_cmp_eq_u32 s99, s66\n\t"
      "s_cselect_b32 s64, s65, s99\n\t"
      "v_lshlrev_b32 v64, 16, v160\n\t"
      "v_lshlrev_b32 v65, 16, v164\n\t"
      "v_and_b32 v66, 0xffff0000, v160\n\t"
      "v_and_b32 v67, 0xffff0000, v164\n\t"
      "v_lshlrev_b32 v68, 16, v161\n\t"
      "v_lshlrev_b32 v69, 16, v165\n\t"
      "v_and_b32 v70, 0xffff0000, v161\n\t"
      "v_and_b32 v71, 0xffff0000, v165\n\t"
      "v_lshlrev_b32 v72, 16, v162\n\t"
      "v_lshlrev_b32 v73, 16, v166\n\t"
      "v_and_b32 v74, 0xffff0000, v162\n\t"
      "v_and_b32 v75, 0xffff0000, v166\n\t"
      "v_lshlrev_b32 v76, 16, v163\n\t"
      "v_lshlrev_b32 v77, 16, v167\n\t"
      "v_and_b32 v78, 0xffff0000, v163\n\t"
      "v_and_b32 v79, 0xffff0000, v167\n\t"
      "v_pk_add_f32 v[80:81], v[64:65], v[66:67]\n\t"
      "v_pk_add_f32 v[82:83], v[68:69], v[70:71]\n\t"
      "v_pk_add_f32 v[84:85], v[72:73], v[74:75]\n\t"
      "v_pk_add_f32 v[86:87], v[76:77], v[78:79]\n\t"
      "v_pk_add_f32 v[80:81], v[80:81], v[82:83]\n\t"
      "v_pk_add_f32 v[84:85], v[84:85], v[86:87]\n\t"
      "v_pk_add_f32 v[80:81], v[80:81], v[84:85]\n\t"
      "s_nop 0\n\t"
      "s_nop 0\n\t"
      "v_add_f32_dpp v80, v80, v80 row_shr:1 row_mask:0xf bank_mask:0xf bound_ctrl:1\n\t"
      "v_add_f32_dpp v81, v81, v81 row_shr:1 row_mask:0xf bank_mask:0xf bound_ctrl:1\n\t"
      "s_nop 0\n\t"
      "v_add_f32_dpp v80, v80, v80 row_shr:2 row_mask:0xf bank_mask:0xf bound_ctrl:1\n\t"
      "v_add_f32_dpp v81, v81, v81 row_shr:2 row_mask:0xf bank_mask:0xf bound_ctrl:1\n\t"
      "s_nop 0\n\t"
      "v_add_f32_dpp v80, v80, v80 row_shr:4 row_mask:0xf bank_mask:0xf bound_ctrl:1\n\t"
      "v_add_f32_dpp v81, v81, v81 row_shr:4 row_mask:0xf bank_mask:0xf bound_ctrl:1\n\t"
      "s_nop 0\n\t"
      "v_add_f32_dpp v80, v80, v80 row_shr:8 row_mask:0xf bank_mask:0xf bound_ctrl:1\n\t"
      "v_add_f32_dpp v81, v81, v81 row_shr:8 row_mask:0xf bank_mask:0xf bound_ctrl:1\n\t"
      "s_nop 0\n\t"
      "v_add_f32_dpp v80, v80, v80 row_bcast:15 row_mask:0xa bank_mask:0xf\n\t"
      "v_add_f32_dpp v81, v81, v81 row_bcast:15 row_mask:0xa bank_mask:0xf\n\t"
      "s_nop 0\n\t"
      "v_add_f32_dpp v80, v80, v80 row_bcast:31 row_mask:0xc bank_mask:0xf\n\t"
      "v_add_f32_dpp v81, v81, v81 row_bcast:31 row_mask:0xc bank_mask:0xf\n\t"
      "s_nop 0\n\t"
      "v_readlane_b32 s72, v80, 63\n\t"
      "v_readlane_b32 s73, v81, 63\n\t"
      "s_nop 1\n\t"
      "v_mov_b32 v90, s72\n\t"
      "v_mov_b32 v91, s73\n\t"
      "v_mul_f32 v90, 0xbb000000, v90\n\t"
      "v_mul_f32 v91, 0xbb000000, v91\n\t"
      "v_pk_add_f32 v[64:65], v[64:65], v[90:91]\n\t"
      "v_pk_add_f32 v[66:67], v[66:67], v[90:91]\n\t"
      "v_pk_add_f32 v[68:69], v[68:69], v[90:91]\n\t"
      "v_pk_add_f32 v[70:71], v[70:71], v[90:91]\n\t"
      "v_pk_add_f32 v[72:73], v[72:73], v[90:91]\n\t"
      "v_pk_add_f32 v[74:75], v[74:75], v[90:91]\n\t"
      "v_pk_add_f32 v[76:77], v[76:77], v[90:91]\n\t"
      "v_pk_add_f32 v[78:79], v[78:79], v[90:91]\n\t"
      "v_pk_mul_f32 v[80:81], v[64:65], v[64:65]\n\t"
      "v_pk_mul_f32 v[82:83], v[66:67], v[66:67]\n\t"
      "v_pk_fma_f32 v[80:81], v[68:69], v[68:69], v[80:81]\n\t"
      "v_pk_fma_f32 v[82:83], v[70:71], v[70:71], v[82:83]\n\t"
      "v_pk_fma_f32 v[80:81], v[72:73], v[72:73], v[80:81]\n\t"
      "v_pk_fma_f32 v[82:83], v[74:75], v[74:75], v[82:83]\n\t"
      "v_pk_fma_f32 v[80:81], v[76:77], v[76:77], v[80:81]\n\t"
      "v_pk_fma_f32 v[82:83], v[78:79], v[78:79], v[82:83]\n\t"
      "v_pk_add_f32 v[80:81], v[80:81], v[82:83]\n\t"
      "s_nop 0\n\t"
      "s_nop 0\n\t"
      "v_add_f32_dpp v80, v80, v80 row_shr:1 row_mask:0xf bank_mask:0xf bound_ctrl:1\n\t"
      "v_add_f32_dpp v81, v81, v81 row_shr:1 row_mask:0xf bank_mask:0xf bound_ctrl:1\n\t"
      "s_nop 0\n\t"
      "v_add_f32_dpp v80, v80, v80 row_shr:2 row_mask:0xf bank_mask:0xf bound_ctrl:1\n\t"
      "v_add_f32_dpp v81, v81, v81 row_shr:2 row_mask:0xf bank_mask:0xf bound_ctrl:1\n\t"
      "s_nop 0\n\t"
      "v_add_f32_dpp v80, v80, v80 row_shr:4 row_mask:0xf bank_mask:0xf bound_ctrl:1\n\t"
      "v_add_f32_dpp v81, v81, v81 row_shr:4 row_mask:0xf bank_mask:0xf bound_ctrl:1\n\t"
      "s_nop 0\n\t"
      "v_add_f32_dpp v80, v80, v80 row_shr:8 row_mask:0xf bank_mask:0xf bound_ctrl:1\n\t"
      "v_add_f32_dpp v81, v81, v81 row_shr:8 row_mask:0xf bank_mask:0xf bound_ctrl:1\n\t"
      "s_nop 0\n\t"
      "v_add_f32_dpp v80, v80, v80 row_bcast:15 row_mask:0xa bank_mask:0xf\n\t"
      "v_add_f32_dpp v81, v81, v81 row_bcast:15 row_mask:0xa bank_mask:0xf\n\t"
      "s_nop 0\n\t"
      "v_add_f32_dpp v80, v80, v80 row_bcast:31 row_mask:0xc bank_mask:0xf\n\t"
      "v_add_f32_dpp v81, v81, v81 row_bcast:31 row_mask:0xc bank_mask:0xf\n\t"
      "s_nop 0\n\t"
      "v_readlane_b32 s72, v80, 63\n\t"
      "v_readlane_b32 s73, v81, 63\n\t"
      "s_nop 1\n\t"
      "v_mov_b32 v90, s72\n\t"
      "v_mov_b32 v91, s73\n\t"
      "v_mov_b32 v92, 0x3727c5ac\n\t"
      "v_fmac_f32 v92, 0x3b000000, v90\n\t"
      "v_mov_b32 v93, 0x3727c5ac\n\t"
      "v_fmac_f32 v93, 0x3b000000, v91\n\t"
      "v_rsq_f32 v90, v92\n\t"
      "v_rsq_f32 v91, v93\n\t"
      "s_nop 0\n\t"
      "v_pk_mul_f32 v[64:65], v[64:65], v[90:91]\n\t"
      "v_pk_mul_f32 v[66:67], v[66:67], v[90:91]\n\t"
      "v_pk_mul_f32 v[68:69], v[68:69], v[90:91]\n\t"
      "v_pk_mul_f32 v[70:71], v[70:71], v[90:91]\n\t"
      "v_pk_mul_f32 v[72:73], v[72:73], v[90:91]\n\t"
      "v_pk_mul_f32 v[74:75], v[74:75], v[90:91]\n\t"
      "v_pk_mul_f32 v[76:77], v[76:77], v[90:91]\n\t"
      "v_pk_mul_f32 v[78:79], v[78:79], v[90:91]\n\t"
      "v_cvt_pk_bf16_f32 v80, v64, v65\n\t"
      "v_cvt_pk_bf16_f32 v81, v66, v67\n\t"
      "v_cvt_pk_bf16_f32 v82, v68, v69\n\t"
      "v_cvt_pk_bf16_f32 v83, v70, v71\n\t"
      "v_cvt_pk_bf16_f32 v84, v72, v73\n\t"
      "v_cvt_pk_bf16_f32 v85, v74, v75\n\t"
      "v_cvt_pk_bf16_f32 v86, v76, v77\n\t"
      "v_cvt_pk_bf16_f32 v87, v78, v79\n\t"
      "ds_write_b32 v192, v80 offset:0\n\t"
      "ds_write_b32 v193, v81 offset:0\n\t"
      "ds_write_b32 v194, v82 offset:0\n\t"
      "ds_write_b32 v195, v83 offset:0\n\t"
      "ds_write_b32 v192, v84 offset:128\n\t"
      "ds_write_b32 v193, v85 offset:128\n\t"
      "ds_write_b32 v194, v86 offset:128\n\t"
      "ds_write_b32 v195, v87 offset:128\n\t"
      "s_waitcnt lgkmcnt(0)\n\t"
      "s_barrier\n\t"
      "ds_read_b128 v[64:67], v196\n\t"
      "ds_read_b128 v[68:71], v196 offset:4096\n\t"
      "s_waitcnt lgkmcnt(1)\n\t"
      "buffer_store_dwordx4 v[64:67], v197, s[44:47], s76 offen sc1\n\t"
      "s_waitcnt lgkmcnt(0)\n\t"
      "buffer_store_dwordx4 v[68:71], v197, s[44:47], s77 offen sc1\n\t"
      "s_add_u32 m0, s70, 8192\n\t"
      "s_add_u32 s99, s64, 57344\n\t"
      "buffer_load_dwordx4 v130, s[36:39], s64 offen lds\n\t"
      "buffer_load_dwordx4 v131, s[36:39], s64 offen offset:1024 lds\n\t"
      "s_cmp_eq_u32 s99, s66\n\t"
      "s_cselect_b32 s64, s65, s99\n\t"
      "s_add_u32 m0, s70, 10240\n\t"
      "s_add_u32 s99, s64, 57344\n\t"
      "buffer_load_dwordx4 v130, s[36:39], s64 offen lds\n\t"
      "buffer_load_dwordx4 v131, s[36:39], s64 offen offset:1024 lds\n\t"
      "s_cmp_eq_u32 s99, s66\n\t"
      "s_cselect_b32 s64, s65, s99\n\t"
      "s_waitcnt vmcnt(17)\n\t"
      "ds_read_b64_tr_b16 a[0:1], v134 offset:0\n\t"
      "ds_read_b64_tr_b16 a[2:3], v134 offset:512\n\t"
      "ds_read_b64_tr_b16 a[4:5], v135 offset:0\n\t"
      "ds_read_b64_tr_b16 a[6:7], v135 offset:512\n\t"
      "s_add_u32 m0, s70, 12288\n\t"
      "s_add_u32 s99, s64, 57344\n\t"
      "buffer_load_dwordx4 v130, s[36:39], s64 offen lds\n\t"
      "buffer_load_dwordx4 v131, s[36:39], s64 offen offset:1024 lds\n\t"
      "s_cmp_eq_u32 s99, s66\n\t"
      "s_cselect_b32 s64, s65, s99\n\t"
      "s_add_u32 m0, s70, 14336\n\t"
      "s_add_u32 s99, s64, 57344\n\t"
      "buffer_load_dwordx4 v130, s[36:39], s64 offen lds\n\t"
      "buffer_load_dwordx4 v131, s[36:39], s64 offen offset:1024 lds\n\t"
      "s_cmp_eq_u32 s99, s66\n\t"
      "s_cselect_b32 s64, s65, s99\n\t"
      "s_add_u32 m0, s70, 16384\n\t"
      "s_add_u32 s99, s64, 57344\n\t"
      "buffer_load_dwordx4 v130, s[36:39], s64 offen lds\n\t"
      "buffer_load_dwordx4 v131, s[36:39], s64 offen offset:1024 lds\n\t"
      "s_cmp_eq_u32 s99, s66\n\t"
      "s_cselect_b32 s64, s65, s99\n\t"
      "s_add_u32 m0, s70, 18432\n\t"
      "s_add_u32 s99, s64, 57344\n\t"
      "buffer_load_dwordx4 v130, s[36:39], s64 offen lds\n\t"
      "buffer_load_dwordx4 v131, s[36:39], s64 offen offset:1024 lds\n\t"
      "s_cmp_eq_u32 s99, s66\n\t"
      "s_cselect_b32 s64, s65, s99\n\t"
      "s_waitcnt vmcnt(23)\n\t"
      "ds_read_b64_tr_b16 a[8:9], v134 offset:2048\n\t"
      "ds_read_b64_tr_b16 a[10:11], v134 offset:2560\n\t"
      "ds_read_b64_tr_b16 a[12:13], v135 offset:2048\n\t"
      "ds_read_b64_tr_b16 a[14:15], v135 offset:2560\n\t"
      "s_add_u32 m0, s70, 20480\n\t"
      "s_add_u32 s99, s64, 57344\n\t"
      "buffer_load_dwordx4 v130, s[36:39], s64 offen lds\n\t"
      "buffer_load_dwordx4 v131, s[36:39], s64 offen offset:1024 lds\n\t"
      "s_cmp_eq_u32 s99, s66\n\t"
      "s_cselect_b32 s64, s65, s99\n\t"
      "s_waitcnt vmcnt(18)\n\t"
      "ds_read_b64_tr_b16 a[16:17], v134 offset:4096\n\t"
      "ds_read_b64_tr_b16 a[18:19], v134 offset:4608\n\t"
      "ds_read_b64_tr_b16 a[20:21], v135 offset:4096\n\t"
      "ds_read_b64_tr_b16 a[22:23], v135 offset:4608\n\t"
      "s_add_u32 m0, s70, 22528\n\t"
      "s_add_u32 s99, s64, 57344\n\t"
      "buffer_load_dwordx4 v130, s[36:39], s64 offen lds\n\t"
      "buffer_load_dwordx4 v131, s[36:39], s64 offen offset:1024 lds\n\t"
      "s_cmp_eq_u32 s99, s66\n\t"
      "s_cselect_b32 s64, s65, s99\n\t"
      "s_waitcnt vmcnt(18)\n\t"
      "s_waitcnt lgkmcnt(4)\n\t"
      "ds_read_b64_tr_b16 a[24:25], v134 offset:6144\n\t"
      "ds_read_b64_tr_b16 a[26:27], v134 offset:6656\n\t"
      "ds_read_b64_tr_b16 a[28:29], v135 offset:6144\n\t"
      "ds_read_b64_tr_b16 a[30:31], v135 offset:6656\n\t"
      "s_add_u32 m0, s70, 24576\n\t"
      "s_add_u32 s99, s64, 57344\n\t"
      "buffer_load_dwordx4 v130, s[36:39], s64 offen lds\n\t"
      "buffer_load_dwordx4 v131, s[36:39], s64 offen offset:1024 lds\n\t"
      "s_cmp_eq_u32 s99, s66\n\t"
      "s_cselect_b32 s64, s65, s99\n\t"
      "s_waitcnt vmcnt(18)\n\t"
      "v_add_co_u32 v138, vcc, 1, v138\n\t"
      "s_nop 1\n\t"
      "v_addc_co_u32 v139, vcc, 0, v139, vcc\n\t"
      "s_barrier\n\t"
      "s_cmp_lg_u32 s56, 0\n\t"
      "s_cbranch_scc1 Lnofl2_%=\n\t"
      "s_mov_b64 exec, 1\n\t"
      "global_store_dwordx2 v153, v[138:139], s[34:35] sc1\n\t"
      "s_mov_b64 exec, -1\n\t"
      "Lnofl2_%=:\n\t"
      "v_lshlrev_b32 v176, 16, v140\n\t"
      "v_and_b32 v177, 0xffff0000, v140\n\t"
      "v_lshlrev_b32 v178, 16, v141\n\t"
      "v_and_b32 v179, 0xffff0000, v141\n\t"
      "v_lshlrev_b32 v180, 16, v142\n\t"
      "v_and_b32 v181, 0xffff0000, v142\n\t"
      "v_lshlrev_b32 v182, 16, v143\n\t"
      "v_and_b32 v183, 0xffff0000, v143\n\t"
      "v_lshlrev_b32 v184, 16, v144\n\t"
      "v_and_b32 v185, 0xffff0000, v144\n\t"
      "v_lshlrev_b32 v186, 16, v145\n\t"
      "v_and_b32 v187, 0xffff0000, v145\n\t"
      "v_lshlrev_b32 v188, 16, v146\n\t"
      "v_and_b32 v189, 0xffff0000, v146\n\t"
      "v_lshlrev_b32 v190, 16, v147\n\t"
      "v_and_b32 v191, 0xffff0000, v147\n\t"
      "v_mov_b32 v204, 0xc0135761\n\t"
      "v_mov_b32 v205, 0xc0135761\n\t"
      "v_mov_b32 v206, 0xbdd2d3e8\n\t"
      "v_mov_b32 v207, 0xbdd2d3e8\n\t"
      "v_mov_b32 v208, 1.0\n\t"
      "v_mov_b32 v209, 1.0\n\t"
      "s_lshl_b32 s72, s61, 5\n\t"
      "s_add_u32 s72, s72, s60\n\t"
      "s_lshl_b32 s72, s72, 7\n\t"
      "s_add_u32 s72, s72, 36864\n\t"
      "v_mov_b32 v153, s72\n\t"
      "s_waitcnt vmcnt(16)\n\t"
      "ds_read_b64_tr_b16 a[32:33], v134 offset:8192\n\t"
      "ds_read_b64_tr_b16 a[34:35], v134 offset:8704\n\t"
      "ds_read_b64_tr_b16 a[36:37], v135 offset:8192\n\t"
      "ds_read_b64_tr_b16 a[38:39], v135 offset:8704\n\t"
      "s_add_u32 m0, s70, 26624\n\t"
      "s_add_u32 s99, s64, 57344\n\t"
      "buffer_load_dwordx4 v130, s[36:39], s64 offen lds\n\t"
      "buffer_load_dwordx4 v131, s[36:39], s64 offen offset:1024 lds\n\t"
      "s_cmp_eq_u32 s99, s66\n\t"
      "s_cselect_b32 s64, s65, s99\n\t"
      "s_waitcnt vmcnt(16)\n\t"
      "s_waitcnt lgkmcnt(4)\n\t"
      "ds_read_b64_tr_b16 a[40:41], v134 offset:10240\n\t"
      "ds_read_b64_tr_b16 a[42:43], v134 offset:10752\n\t"
      "ds_read_b64_tr_b16 a[44:45], v135 offset:10240\n\t"
      "ds_read_b64_tr_b16 a[46:47], v135 offset:10752\n\t"
      "s_add_u32 m0, s70, 0\n\t"
      "s_add_u32 s99, s64, 57344\n\t"
      "buffer_load_dwordx4 v130, s[36:39], s64 offen lds\n\t"
      "buffer_load_dwordx4 v131, s[36:39], s64 offen offset:1024 lds\n\t"
      "s_cmp_eq_u32 s99, s66\n\t"
      "s_cselect_b32 s64, s65, s99\n\t"
      "v_lshrrev_b32 v168, 3, v128\n\t"
      "v_bfe_u32 v169, v128, 4, 1\n\t"
      "v_lshlrev_b32 v169, 2, v169\n\t"
      "v_and_b32 v170, 7, v128\n\t"
      "v_xor_b32 v170, v170, v169\n\t"
      "v_lshlrev_b32 v170, 4, v170\n\t"
      "v_bfe_u32 v169, v128, 5, 1\n\t"
      "v_lshlrev_b32 v169, 3, v169\n\t"
      "v_and_b32 v171, 3, v168\n\t"
      "v_add_u32 v171, v171, v169\n\t"
      "v_lshlrev_b32 v171, 12, v171\n\t"
      "v_add_u32 v132, v171, v170\n\t"
      "v_add_u32 v133, 15360, v132\n\t"
      "s_mov_b32 s40, s26\n\t"
      "s_and_b32 s41, s27, 0xffff\n\t"
      "s_mov_b32 s42, 7340032\n\t"
      "s_mov_b32 s43, 0x20000\n\t"
      "s_mul_i32 s68, s56, 1835008\n\t"
      "s_lshl_b32 s72, s62, 7\n\t"
      "s_add_u32 s68, s68, s72\n\t"
      "s_add_u32 s69, s68, 1835008\n\t"
      "s_mul_i32 s72, s63, 7\n\t"
      "s_lshr_b32 s72, s72, 1\n\t"
      "s_mul_i32 s72, s72, 65536\n\t"
      "s_add_u32 s67, s68, s72\n\t"
      "s_waitcnt vmcnt(16)\n\t"
      "ds_read_b64_tr_b16 a[48:49], v134 offset:12288\n\t"
      "ds_read_b64_tr_b16 a[50:51], v134 offset:12800\n\t"
      "ds_read_b64_tr_b16 a[52:53], v135 offset:12288\n\t"
      "ds_read_b64_tr_b16 a[54:55], v135 offset:12800\n\t"
      "s_add_u32 m0, s70, 2048\n\t"
      "s_add_u32 s99, s64, 57344\n\t"
      "buffer_load_dwordx4 v130, s[36:39], s64 offen lds\n\t"
      "buffer_load_dwordx4 v131, s[36:39], s64 offen offset:1024 lds\n\t"
      "s_cmp_eq_u32 s99, s66\n\t"
      "s_cselect_b32 s64, s65, s99\n\t"
      "s_waitcnt vmcnt(16)\n\t"
      "s_waitcnt lgkmcnt(4)\n\t"
      "ds_read_b64_tr_b16 a[56:57], v134 offset:14336\n\t"
      "ds_read_b64_tr_b16 a[58:59], v134 offset:14848\n\t"
      "ds_read_b64_tr_b16 a[60:61], v135 offset:14336\n\t"
      "ds_read_b64_tr_b16 a[62:63], v135 offset:14848\n\t"
      "s_add_u32 m0, s70, 4096\n\t"
      "s_add_u32 s99, s64, 57344\n\t"
      "buffer_load_dwordx4 v130, s[36:39], s64 offen lds\n\t"
      "buffer_load_dwordx4 v131, s[36:39], s64 offen offset:1024 lds\n\t"
      "s_cmp_eq_u32 s99, s66\n\t"
      "s_cselect_b32 s64, s65, s99\n\t"
      "s_lshl_b32 s72, s78, 10\n\t"
      "v_add_u32 v136, s72, v154\n\t"
      "s_xor_b32 s72, s72, 1024\n\t"
      "v_add_u32 v137, s72, v154\n\t"
      "s_lshl_b32 s72, s56, 11\n\t"
      "s_add_u32 s72, s72, 4096\n\t"
      "v_lshlrev_b32 v198, 5, v128\n\t"
      "v_add_u32 v198, s72, v198\n\t"
      "s_add_u32 s48, s34, 2228224\n\t"
      "s_addc_u32 s49, s35, 0\n\t"
      "s_and_b32 s49, s49, 0xffff\n\t"
      "s_mov_b32 s50, 1835008\n\t"
      "s_mov_b32 s51, 0x20000\n\t"
      "s_mov_b32 s52, s30\n\t"
      "s_and_b32 s53, s31, 0xffff\n\t"
      "s_mov_b32 s54, 2097152\n\t"
      "s_mov_b32 s55, 0x20000\n\t"
      "s_waitcnt vmcnt(16)\n\t"
      "ds_read_b64_tr_b16 a[64:65], v134 offset:16384\n\t"
      "ds_read_b64_tr_b16 a[66:67], v134 offset:16896\n\t"
      "ds_read_b64_tr_b16 a[68:69], v135 offset:16384\n\t"
      "ds_read_b64_tr_b16 a[70:71], v135 offset:16896\n\t"
      "s_add_u32 m0, s70, 6144\n\t"
      "s_add_u32 s99, s64, 57344\n\t"
      "buffer_load_dwordx4 v130, s[36:39], s64 offen lds\n\t"
      "buffer_load_dwordx4 v131, s[36:39], s64 offen offset:1024 lds\n\t"
      "s_cmp_eq_u32 s99, s66\n\t"
      "s_cselect_b32 s64, s65, s99\n\t"
      "s_waitcnt vmcnt(16)\n\t"
      "s_waitcnt lgkmcnt(4)\n\t"
      "ds_read_b64_tr_b16 a[72:73], v134 offset:18432\n\t"
      "ds_read_b64_tr_b16 a[74:75], v134 offset:18944\n\t"
      "ds_read_b64_tr_b16 a[76:77], v135 offset:18432\n\t"
      "ds_read_b64_tr_b16 a[78:79], v135 offset:18944\n\t"
      "s_add_u32 m0, s70, 8192\n\t"
      "s_add_u32 s99, s64, 57344\n\t"
      "buffer_load_dwordx4 v130, s[36:39], s64 offen lds\n\t"
      "buffer_load_dwordx4 v131, s[36:39], s64 offen offset:1024 lds\n\t"
      "s_cmp_eq_u32 s99, s66\n\t"
      "s_cselect_b32 s64, s65, s99\n\t"
      "s_xor_b32 s72, s56, 1\n\t"
      "s_mul_i32 s72, s72, 12288\n\t"
      "s_add_u32 s72, s72, 114688\n\t"
      "s_add_u32 s72, s72, %[ldsb]\n\t"
      "v_add_u32 v149, s72, v154\n\t"
      "s_xor_b32 s72, s56, 2\n\t"
      "s_mul_i32 s72, s72, 12288\n\t"
      "s_add_u32 s72, s72, 118784\n\t"
      "s_add_u32 s72, s72, %[ldsb]\n\t"
      "v_add_u32 v150, s72, v154\n\t"
      "s_xor_b32 s72, s56, 3\n\t"
      "s_mul_i32 s72, s72, 12288\n\t"
      "s_add_u32 s72, s72, 122880\n\t"
      "s_add_u32 s72, s72, %[ldsb]\n\t"
      "v_add_u32 v151, s72, v154\n\t"
      "s_mul_i32 s72, s56, 12288\n\t"
      "s_add_u32 s72, s72, 114688\n\t"
      "s_add_u32 s72, s72, %[ldsb]\n\t"
      "v_add_u32 v152, s72, v154\n\t"
      "s_lshl_b32 s72, s62, 6\n\t"
      "s_lshl_b32 s73, s79, 5\n\t"
      "s_add_u32 s72, s72, s73\n\t"
      "s_lshl_b32 s72, s72, 10\n\t"
      "s_lshl_b32 s73, s63, 7\n\t"
      "s_add_u32 s72, s72, s73\n\t"
      "s_lshl_b32 s73, s78, 6\n\t"
      "s_add_u32 s95, s72, s73\n\t"
      "s_lshl_b32 s72, s62, 6\n\t"
      "s_lshl_b32 s73, s56, 4\n\t"
      "s_add_u32 s72, s72, s73\n\t"
      "s_lshl_b32 s72, s72, 10\n\t"
      "s_lshl_b32 s73, s63, 7\n\t"
      "s_add_u32 s95, s72, s73\n\t"
      "v_lshrrev_b32 v168, 3, v128\n\t"
      "v_and_b32 v169, 7, v128\n\t"
      "v_lshlrev_b32 v169, 4, v169\n\t"
      "v_lshl_add_u32 v155, v168, 10, v169\n\t"
      "s_mul_i32 s72, s56, 2304\n\t"
      "s_add_u32 s72, s72, %[ldsb]\n\t"
      "v_mul_u32_u24 v168, 144, v168\n\t"
      "v_add3_u32 v203, v168, v169, s72\n\t"
      "s_mul_i32 s72, s79, 4608\n\t"
      "s_lshl_b32 s73, s78, 6\n\t"
      "s_add_u32 s72, s72, s73\n\t"
      "s_add_u32 s72, s72, %[ldsb]\n\t"
      "v_and_b32 v168, 31, v128\n\t"
      "v_mul_u32_u24 v168, 144, v168\n\t"
      "v_lshrrev_b32 v169, 5, v128\n\t"
      "v_lshlrev_b32 v169, 5, v169\n\t"
      "v_add3_u32 v202, v168, v169, s72\n\t"
      "s_waitcnt vmcnt(16)\n\t"
      "ds_read_b64_tr_b16 a[80:81], v134 offset:20480\n\t"
      "ds_read_b64_tr_b16 a[82:83], v134 offset:20992\n\t"
      "ds_read_b64_tr_b16 a[84:85], v135 offset:20480\n\t"
      "ds_read_b64_tr_b16 a[86:87], v135 offset:20992\n\t"
      "s_add_u32 m0, s70, 10240\n\t"
      "s_add_u32 s99, s64, 57344\n\t"
      "buffer_load_dwordx4 v130, s[36:39], s64 offen lds\n\t"
      "buffer_load_dwordx4 v131, s[36:39], s64 offen offset:1024 lds\n\t"
      "s_cmp_eq_u32 s99, s66\n\t"
      "s_cselect_b32 s64, s65, s99\n\t"
      "s_waitcnt vmcnt(16)\n\t"
      "s_waitcnt lgkmcnt(4)\n\t"
      "ds_read_b64_tr_b16 a[88:89], v134 offset:22528\n\t"
      "ds_read_b64_tr_b16 a[90:91], v134 offset:23040\n\t"
      "ds_read_b64_tr_b16 a[92:93], v135 offset:22528\n\t"
      "ds_read_b64_tr_b16 a[94:95], v135 offset:23040\n\t"
      "s_add_u32 m0, s70, 12288\n\t"
      "s_add_u32 s99, s64, 57344\n\t"
      "buffer_load_dwordx4 v130, s[36:39], s64 offen lds\n\t"
      "buffer_load_dwordx4 v131, s[36:39], s64 offen offset:1024 lds\n\t"
      "s_cmp_eq_u32 s99, s66\n\t"
      "s_cselect_b32 s64, s65, s99\n\t"
      "s_mul_i32 s92, s61, 128\n\t"
      "s_mul_i32 s72, s56, 32\n\t"
      "s_add_u32 s92, s92, s72\n\t"
      "s_lshl_b32 s92, s92, 11\n\t"
      "s_add_u32 s93, s92, 65536\n\t"
      "s_lshl_b32 s72, s61, 2\n\t"
      "s_and_b32 s74, s61, 3\n\t"
      "s_lshl_b32 s74, s74, 3\n\t"
      "s_cmp_ge_u32 s58, 24\n\t"
      "s_cselect_b32 s72, s74, s72\n\t"
      "s_lshl_b32 s72, s72, 11\n\t"
      "s_add_u32 s91, s92, s72\n\t"
      "s_mul_i32 s72, s61, 112\n\t"
      "s_lshl_b32 s73, s60, 2\n\t"
      "s_add_u32 s72, s72, s73\n\t"
      "s_lshl_b32 s73, s79, 1\n\t"
      "s_add_u32 s72, s72, s73\n\t"
      "s_lshl_b32 s72, s72, 1\n\t"
      "s_add_u32 s72, s72, s78\n\t"
      "s_lshl_b32 s71, s72, 10\n\t"
      "s_waitcnt vmcnt(16)\n\t"
      "ds_read_b64_tr_b16 a[96:97], v134 offset:24576\n\t"
      "ds_read_b64_tr_b16 a[98:99], v134 offset:25088\n\t"
      "ds_read_b64_tr_b16 a[100:101], v135 offset:24576\n\t"
      "ds_read_b64_tr_b16 a[102:103], v135 offset:25088\n\t"
      "s_add_u32 m0, s70, 14336\n\t"
      "s_add_u32 s99, s64, 57344\n\t"
      "buffer_load_dwordx4 v130, s[36:39], s64 offen lds\n\t"
      "buffer_load_dwordx4 v131, s[36:39], s64 offen offset:1024 lds\n\t"
      "s_cmp_eq_u32 s99, s66\n\t"
      "s_cselect_b32 s64, s65, s99\n\t"
      "s_waitcnt vmcnt(16)\n\t"
      "s_waitcnt lgkmcnt(4)\n\t"
      "ds_read_b64_tr_b16 a[104:105], v134 offset:26624\n\t"
      "ds_read_b64_tr_b16 a[106:107], v134 offset:27136\n\t"
      "ds_read_b64_tr_b16 a[108:109], v135 offset:26624\n\t"
      "ds_read_b64_tr_b16 a[110:111], v135 offset:27136\n\t"
      "s_add_u32 m0, s70, 16384\n\t"
      "s_add_u32 s99, s64, 57344\n\t"
      "buffer_load_dwordx4 v130, s[36:39], s64 offen lds\n\t"
      "buffer_load_dwordx4 v131, s[36:39], s64 offen offset:1024 lds\n\t"
      "s_cmp_eq_u32 s99, s66\n\t"
      "s_cselect_b32 s64, s65, s99\n\t"
      "s_waitcnt vmcnt(16)\n\t"
      "ds_read_b64_tr_b16 a[112:113], v134 offset:0\n\t"
      "ds_read_b64_tr_b16 a[114:115], v134 offset:512\n\t"
      "ds_read_b64_tr_b16 a[116:117], v135 offset:0\n\t"
      "ds_read_b64_tr_b16 a[118:119], v135 offset:512\n\t"
      "s_add_u32 m0, s70, 18432\n\t"
      "s_add_u32 s99, s64, 57344\n\t"
      "buffer_load_dwordx4 v130, s[36:39], s64 offen lds\n\t"
      "buffer_load_dwordx4 v131, s[36:39], s64 offen offset:1024 lds\n\t"
      "s_cmp_eq_u32 s99, s66\n\t"
      "s_cselect_b32 s64, s65, s99\n\t"
      "s_waitcnt vmcnt(16)\n\t"
      "s_waitcnt lgkmcnt(4)\n\t"
      "ds_read_b64_tr_b16 a[120:121], v134 offset:2048\n\t"
      "ds_read_b64_tr_b16 a[122:123], v134 offset:2560\n\t"
      "ds_read_b64_tr_b16 a[124:125], v135 offset:2048\n\t"
      "ds_read_b64_tr_b16 a[126:127], v135 offset:2560\n\t"
      "s_add_u32 m0, s70, 20480\n\t"
      "s_add_u32 s99, s64, 57344\n\t"
      "buffer_load_dwordx4 v130, s[36:39], s64 offen lds\n\t"
      "buffer_load_dwordx4 v131, s[36:39], s64 offen offset:1024 lds\n\t"
      "s_cmp_eq_u32 s99, s66\n\t"
      "s_cselect_b32 s64, s65, s99\n\t"
      "s_waitcnt vmcnt(16)\n\t"
      "ds_read_b64_tr_b16 a[128:129], v134 offset:4096\n\t"
      "ds_read_b64_tr_b16 a[130:131], v134 offset:4608\n\t"
      "ds_read_b64_tr_b16 a[132:133], v135 offset:4096\n\t"
      "ds_read_b64_tr_b16 a[134:135], v135 offset:4608\n\t"
      "s_add_u32 m0, s70, 22528\n\t"
      "s_add_u32 s99, s64, 57344\n\t"
      "buffer_load_dwordx4 v130, s[36:39], s64 offen lds\n\t"
      "buffer_load_dwordx4 v131, s[36:39], s64 offen offset:1024 lds\n\t"
      "s_cmp_eq_u32 s99, s66\n\t"
      "s_cselect_b32 s64, s65, s99\n\t"
      "s_waitcnt vmcnt(16)\n\t"
      "s_waitcnt lgkmcnt(4)\n\t"
      "ds_read_b64_tr_b16 a[136:137], v134 offset:6144\n\t"
      "ds_read_b64_tr_b16 a[138:139], v134 offset:6656\n\t"
      "ds_read_b64_tr_b16 a[140:141], v135 offset:6144\n\t"
      "ds_read_b64_tr_b16 a[142:143], v135 offset:6656\n\t"
      "s_add_u32 m0, s70, 24576\n\t"
      "s_add_u32 s99, s64, 57344\n\t"
      "buffer_load_dwordx4 v130, s[36:39], s64 offen lds\n\t"
      "buffer_load_dwordx4 v131, s[36:39], s64 offen offset:1024 lds\n\t"
      "s_cmp_eq_u32 s99, s66\n\t"
      "s_cselect_b32 s64, s65, s99\n\t"
      "s_waitcnt vmcnt(16)\n\t"
      "ds_read_b64_tr_b16 a[144:145], v134 offset:8192\n\t"
      "ds_read_b64_tr_b16 a[146:147], v134 offset:8704\n\t"
      "ds_read_b64_tr_b16 a[148:149], v135 offset:8192\n\t"
      "ds_read_b64_tr_b16 a[150:151], v135 offset:8704\n\t"
      "s_add_u32 m0, s70, 26624\n\t"
      "s_add_u32 s99, s64, 57344\n\t"
      "buffer_load_dwordx4 v130, s[36:39], s64 offen lds\n\t"
      "buffer_load_dwordx4 v131, s[36:39], s64 offen offset:1024 lds\n\t"
      "s_cmp_eq_u32 s99, s66\n\t"
      "s_cselect_b32 s64, s65, s99\n\t"
      "s_waitcnt vmcnt(16)\n\t"
      "s_waitcnt lgkmcnt(4)\n\t"
      "ds_read_b64_tr_b16 a[152:153], v134 offset:10240\n\t"
      "ds_read_b64_tr_b16 a[154:155], v134 offset:10752\n\t"
      "ds_read_b64_tr_b16 a[156:157], v135 offset:10240\n\t"
      "ds_read_b64_tr_b16 a[158:159], v135 offset:10752\n\t"
      "s_add_u32 m0, s70, 0\n\t"
      "s_add_u32 s99, s64, 57344\n\t"
      "buffer_load_dwordx4 v130, s[36:39], s64 offen lds\n\t"
      "buffer_load_dwordx4 v131, s[36:39], s64 offen offset:1024 lds\n\t"
      "s_cmp_eq_u32 s99, s66\n\t"
      "s_cselect_b32 s64, s65, s99\n\t"
      "s_waitcnt vmcnt(16)\n\t"
      "ds_read_b64_tr_b16 a[160:161], v134 offset:12288\n\t"
      "ds_read_b64_tr_b16 a[162:163], v134 offset:12800\n\t"
      "ds_read_b64_tr_b16 a[164:165], v135 offset:12288\n\t"
      "ds_read_b64_tr_b16 a[166:167], v135 offset:12800\n\t"
      "s_add_u32 m0, s70, 2048\n\t"
      "s_add_u32 s99, s64, 57344\n\t"
      "buffer_load_dwordx4 v130, s[36:39], s64 offen lds\n\t"
      "buffer_load_dwordx4 v131, s[36:39], s64 offen offset:1024 lds\n\t"
      "s_cmp_eq_u32 s99, s66\n\t"
      "s_cselect_b32 s64, s65, s99\n\t"
      "s_waitcnt vmcnt(16)\n\t"
      "s_waitcnt lgkmcnt(4)\n\t"
      "ds_read_b64_tr_b16 a[168:169], v134 offset:14336\n\t"
      "ds_read_b64_tr_b16 a[170:171], v134 offset:14848\n\t"
      "ds_read_b64_tr_b16 a[172:173], v135 offset:14336\n\t"
      "ds_read_b64_tr_b16 a[174:175], v135 offset:14848\n\t"
      "s_add_u32 m0, s70, 4096\n\t"
      "s_add_u32 s99, s64, 57344\n\t"
      "buffer_load_dwordx4 v130, s[36:39], s64 offen lds\n\t"
      "buffer_load_dwordx4 v131, s[36:39], s64 offen offset:1024 lds\n\t"
      "s_cmp_eq_u32 s99, s66\n\t"
      "s_cselect_b32 s64, s65, s99\n\t"
      "s_waitcnt vmcnt(16)\n\t"
      "ds_read_b64_tr_b16 a[176:177], v134 offset:16384\n\t"
      "ds_read_b64_tr_b16 a[178:179], v134 offset:16896\n\t"
      "ds_read_b64_tr_b16 a[180:181], v135 offset:16384\n\t"
      "ds_read_b64_tr_b16 a[182:183], v135 offset:16896\n\t"
      "s_add_u32 m0, s70, 6144\n\t"
      "s_add_u32 s99, s64, 57344\n\t"
      "buffer_load_dwordx4 v130, s[36:39], s64 offen lds\n\t"
      "buffer_load_dwordx4 v131, s[36:39], s64 offen offset:1024 lds\n\t"
      "s_cmp_eq_u32 s99, s66\n\t"
      "s_cselect_b32 s64, s65, s99\n\t"
      "global_load_dwordx2 v[200:201], v198, s[34:35] sc1\n\t"
      "s_waitcnt vmcnt(17)\n\t"
      "s_waitcnt lgkmcnt(4)\n\t"
      "ds_read_b64_tr_b16 a[184:185], v134 offset:18432\n\t"
      "ds_read_b64_tr_b16 a[186:187], v134 offset:18944\n\t"
      "ds_read_b64_tr_b16 a[188:189], v135 offset:18432\n\t"
      "ds_read_b64_tr_b16 a[190:191], v135 offset:18944\n\t"
      "s_waitcnt vmcnt(15)\n\t"
      "ds_read_b64_tr_b16 a[192:193], v134 offset:20480\n\t"
      "ds_read_b64_tr_b16 a[194:195], v134 offset:20992\n\t"
      "ds_read_b64_tr_b16 a[196:197], v135 offset:20480\n\t"
      "ds_read_b64_tr_b16 a[198:199], v135 offset:20992\n\t"
      "s_waitcnt vmcnt(13)\n\t"
      "s_waitcnt lgkmcnt(4)\n\t"
      "ds_read_b64_tr_b16 a[200:201], v134 offset:22528\n\t"
      "ds_read_b64_tr_b16 a[202:203], v134 offset:23040\n\t"
      "ds_read_b64_tr_b16 a[204:205], v135 offset:22528\n\t"
      "ds_read_b64_tr_b16 a[206:207], v135 offset:23040\n\t"
      "s_waitcnt vmcnt(11)\n\t"
      "ds_read_b64_tr_b16 a[208:209], v134 offset:24576\n\t"
      "ds_read_b64_tr_b16 a[210:211], v134 offset:25088\n\t"
      "ds_read_b64_tr_b16 a[212:213], v135 offset:24576\n\t"
      "ds_read_b64_tr_b16 a[214:215], v135 offset:25088\n\t"
      "s_waitcnt vmcnt(9)\n\t"
      "s_waitcnt lgkmcnt(4)\n\t"
      "ds_read_b64_tr_b16 a[216:217], v134 offset:26624\n\t"
      "ds_read_b64_tr_b16 a[218:219], v134 offset:27136\n\t"
      "ds_read_b64_tr_b16 a[220:221], v135 offset:26624\n\t"
      "ds_read_b64_tr_b16 a[222:223], v135 offset:27136\n\t"
      "s_waitcnt vmcnt(7)\n\t"
      "ds_read_b64_tr_b16 a[224:225], v134 offset:0\n\t"
      "ds_read_b64_tr_b16 a[226:227], v134 offset:512\n\t"
      "ds_read_b64_tr_b16 a[228:229], v135 offset:0\n\t"
      "ds_read_b64_tr_b16 a[230:231], v135 offset:512\n\t"
      "s_waitcnt vmcnt(0)\n\t"
      "v_cmp_eq_u64 vcc, v[200:201], v[138:139]\n\t"
      "s_nop 1\n\t"
      "s_cmp_eq_u64 vcc, -1\n\t"
      "s_cbranch_scc1 Lchkok3_%=\n\t"
      "s_mov_b32 s94, 0x2000\n\t"
      "Lpoll4_%=:\n\t"
      "global_load_dwordx2 v[200:201], v198, s[34:35] sc1\n\t"
      "s_waitcnt vmcnt(0)\n\t"
      "v_cmp_eq_u64 vcc, v[200:201], v[138:139]\n\t"
      "s_nop 1\n\t"
      "s_cmp_eq_u64 vcc, -1\n\t"
      "s_cbranch_scc1 Lpollok5_%=\n\t"
      "s_sleep 1\n\t"
      "s_sub_u32 s94, s94, 1\n\t"
      "s_cbranch_scc0 Lpoll4_%=\n\t"
      "Lpollok5_%=:\n\t"
      "Lchkok3_%=:\n\t"
      "buffer_load_dwordx4 v[64:67], v136, s[44:47], s91 offen sc1\n\t"
      "buffer_load_dwordx4 v[68:71], v137, s[44:47], s91 offen sc1\n\t"
      "s_add_u32 s91, s91, 2048\n\t"
      "s_cmp_eq_u32 s91, s93\n\t"
      "s_cselect_b32 s91, s92, s91\n\t"
      "buffer_load_dwordx4 v[72:75], v136, s[44:47], s91 offen sc1\n\t"
      "buffer_load_dwordx4 v[76:79], v137, s[44:47], s91 offen sc1\n\t"
      "s_add_u32 s91, s91, 2048\n\t"
      "s_cmp_eq_u32 s91, s93\n\t"
      "s_cselect_b32 s91, s92, s91\n\t"
      "buffer_load_dwordx4 v[80:83], v136, s[44:47], s91 offen sc1\n\t"
      "buffer_load_dwordx4 v[84:87], v137, s[44:47], s91 offen sc1\n\t"
      "s_add_u32 s91, s91, 2048\n\t"
      "s_cmp_eq_u32 s91, s93\n\t"
      "s_cselect_b32 s91, s92, s91\n\t"
      "buffer_load_dwordx4 v[88:91], v136, s[44:47], s91 offen sc1\n\t"
      "buffer_load_dwordx4 v[92:95], v137, s[44:47], s91 offen sc1\n\t"
      "s_add_u32 s91, s91, 2048\n\t"
      "s_cmp_eq_u32 s91, s93\n\t"
      "s_cselect_b32 s91, s92, s91\n\t"
      "buffer_load_dwordx4 v[96:99], v136, s[44:47], s91 offen sc1\n\t"
      "buffer_load_dwordx4 v[100:103], v137, s[44:47], s91 offen sc1\n\t"
      "s_add_u32 s91, s91, 2048\n\t"
      "s_cmp_eq_u32 s91, s93\n\t"
      "s_cselect_b32 s91, s92, s91\n\t"
      "buffer_load_dwordx4 v[104:107], v136, s[44:47], s91 offen sc1\n\t"
      "buffer_load_dwordx4 v[108:111], v137, s[44:47], s91 offen sc1\n\t"
      "s_add_u32 s91, s91, 2048\n\t"
      "s_cmp_eq_u32 s91, s93\n\t"
      "s_cselect_b32 s91, s92, s91\n\t"
      "buffer_load_dwordx4 v[112:115], v136, s[44:47], s91 offen sc1\n\t"
      "buffer_load_dwordx4 v[116:119], v137, s[44:47], s91 offen sc1\n\t"
      "s_add_u32 s91, s91, 2048\n\t"
      "s_cmp_eq_u32 s91, s93\n\t"
      "s_cselect_b32 s91, s92, s91\n\t"
      "buffer_load_dwordx4 v[120:123], v136, s[44:47], s91 offen sc1\n\t"
      "buffer_load_dwordx4 v[124:127], v137, s[44:47], s91 offen sc1\n\t"
      "s_add_u32 s91, s91, 2048\n\t"
      "s_cmp_eq_u32 s91, s93\n\t"
      "s_cselect_b32 s91, s92, s91\n\t"
      "s_waitcnt lgkmcnt(4)\n\t"
      "ds_read_b64_tr_b16 a[232:233], v134 offset:2048\n\t"
      "ds_read_b64_tr_b16 a[234:235], v134 offset:2560\n\t"
      "ds_read_b64_tr_b16 a[236:237], v135 offset:2048\n\t"
      "ds_read_b64_tr_b16 a[238:239], v135 offset:2560\n\t"
      "ds_read_b64_tr_b16 a[240:241], v134 offset:4096\n\t"
      "ds_read_b64_tr_b16 a[242:243], v134 offset:4608\n\t"
      "ds_read_b64_tr_b16 a[244:245], v135 offset:4096\n\t"
      "ds_read_b64_tr_b16 a[246:247], v135 offset:4608\n\t"
      "s_waitcnt lgkmcnt(4)\n\t"
      "ds_read_b64_tr_b16 a[248:249], v134 offset:6144\n\t"
      "ds_read_b64_tr_b16 a[250:251], v134 offset:6656\n\t"
      "ds_read_b64_tr_b16 a[252:253], v135 offset:6144\n\t"
      "ds_read_b64_tr_b16 a[254:255], v135 offset:6656\n\t"
      "s_waitcnt lgkmcnt(0)\n\t"
      "s_waitcnt vmcnt(15)\n\t"
      "v_mfma_f32_32x32x16_bf16 v[0:15], a[0:3], v[64:67], 0\n\t"
      "v_mfma_f32_32x32x16_bf16 v[32:47], a[4:7], v[64:67], 0\n\t"
      "buffer_load_dwordx4 v[64:67], v136, s[44:47], s91 offen sc1\n\t"
      "s_waitcnt vmcnt(15)\n\t"
      "v_mfma_f32_32x32x16_bf16 v[16:31], a[0:3], v[68:71], 0\n\t"
      "v_mfma_f32_32x32x16_bf16 v[48:63], a[4:7], v[68:71], 0\n\t"
      "buffer_load_dwordx4 v[68:71], v137, s[44:47], s91 offen sc1\n\t"
      "s_add_u32 s91, s91, 2048\n\t"
      "s_cmp_eq_u32 s91, s93\n\t"
      "s_cselect_b32 s91, s92, s91\n\t"
      "s_waitcnt vmcnt(15)\n\t"
      "v_mfma_f32_32x32x16_bf16 v[0:15], a[8:11], v[72:75], v[0:15]\n\t"
      "v_mfma_f32_32x32x16_bf16 v[32:47], a[12:15], v[72:75], v[32:47]\n\t"
      "buffer_load_dwordx4 v[72:75], v136, s[44:47], s91 offen sc1\n\t"
      "s_waitcnt vmcnt(15)\n\t"
      "v_mfma_f32_32x32x16_bf16 v[16:31], a[8:11], v[76:79], v[16:31]\n\t"
      "v_mfma_f32_32x32x16_bf16 v[48:63], a[12:15], v[76:79], v[48:63]\n\t"
      "buffer_load_dwordx4 v[76:79], v137, s[44:47], s91 offen sc1\n\t"
      "s_add_u32 s91, s91, 2048\n\t"
      "s_cmp_eq_u32 s91, s93\n\t"
      "s_cselect_b32 s91, s92, s91\n\t"
      "s_waitcnt vmcnt(15)\n\t"
      "v_mfma_f32_32x32x16_bf16 v[0:15], a[16:19], v[80:83], v[0:15]\n\t"
      "v_mfma_f32_32x32x16_bf16 v[32:47], a[20:23], v[80:83], v[32:47]\n\t"
      "buffer_load_dwordx4 v[80:83], v136, s[44:47], s91 offen sc1\n\t"
      "s_waitcnt vmcnt(15)\n\t"
      "v_mfma_f32_32x32x16_bf16 v[16:31], a[16:19], v[84:87], v[16:31]\n\t"
      "v_mfma_f32_32x32x16_bf16 v[48:63], a[20:23], v[84:87], v[48:63]\n\t"
      "buffer_load_dwordx4 v[84:87], v137, s[44:47], s91 offen sc1\n\t"
      "s_add_u32 s91, s91, 2048\n\t"
      "s_cmp_eq_u32 s91, s93\n\t"
      "s_cselect_b32 s91, s92, s91\n\t"
      "s_waitcnt vmcnt(15)\n\t"
      "v_mfma_f32_32x32x16_bf16 v[0:15], a[24:27], v[88:91], v[0:15]\n\t"
      "v_mfma_f32_32x32x16_bf16 v[32:47], a[28:31], v[88:91], v[32:47]\n\t"
      "buffer_load_dwordx4 v[88:91], v136, s[44:47], s91 offen sc1\n\t"
      "s_waitcnt vmcnt(15)\n\t"
      "v_mfma_f32_32x32x16_bf16 v[16:31], a[24:27], v[92:95], v[16:31]\n\t"
      "v_mfma_f32_32x32x16_bf16 v[48:63], a[28:31], v[92:95], v[48:63]\n\t"
      "buffer_load_dwordx4 v[92:95], v137, s[44:47], s91 offen sc1\n\t"
      "s_add_u32 s91, s91, 2048\n\t"
      "s_cmp_eq_u32 s91, s93\n\t"
      "s_cselect_b32 s91, s92, s91\n\t"
      "s_waitcnt vmcnt(15)\n\t"
      "v_mfma_f32_32x32x16_bf16 v[0:15], a[32:35], v[96:99], v[0:15]\n\t"
      "v_mfma_f32_32x32x16_bf16 v[32:47], a[36:39], v[96:99], v[32:47]\n\t"
      "buffer_load_dwordx4 v[96:99], v136, s[44:47], s91 offen sc1\n\t"
      "s_waitcnt vmcnt(15)\n\t"
      "v_mfma_f32_32x32x16_bf16 v[16:31], a[32:35], v[100:103], v[16:31]\n\t"
      "v_mfma_f32_32x32x16_bf16 v[48:63], a[36:39], v[100:103], v[48:63]\n\t"
      "buffer_load_dwordx4 v[100:103], v137, s[44:47], s91 offen sc1\n\t"
      "s_add_u32 s91, s91, 2048\n\t"
      "s_cmp_eq_u32 s91, s93\n\t"
      "s_cselect_b32 s91, s92, s91\n\t"
      "s_waitcnt vmcnt(15)\n\t"
      "v_mfma_f32_32x32x16_bf16 v[0:15], a[40:43], v[104:107], v[0:15]\n\t"
      "v_mfma_f32_32x32x16_bf16 v[32:47], a[44:47], v[104:107], v[32:47]\n\t"
      "buffer_load_dwordx4 v[104:107], v136, s[44:47], s91 offen sc1\n\t"
      "s_waitcnt vmcnt(15)\n\t"
      "v_mfma_f32_32x32x16_bf16 v[16:31], a[40:43], v[108:111], v[16:31]\n\t"
      "v_mfma_f32_32x32x16_bf16 v[48:63], a[44:47], v[108:111], v[48:63]\n\t"
      "buffer_load_dwordx4 v[108:111], v137, s[44:47], s91 offen sc1\n\t"
      "s_add_u32 s91, s91, 2048\n\t"
      "s_cmp_eq_u32 s91, s93\n\t"
      "s_cselect_b32 s91, s92, s91\n\t"
      "s_waitcnt vmcnt(15)\n\t"
      "v_mfma_f32_32x32x16_bf16 v[0:15], a[48:51], v[112:115], v[0:15]\n\t"
      "v_mfma_f32_32x32x16_bf16 v[32:47], a[52:55], v[112:115], v[32:47]\n\t"
      "buffer_load_dwordx4 v[112:115], v136, s[44:47], s91 offen sc1\n\t"
      "s_waitcnt vmcnt(15)\n\t"
      "v_mfma_f32_32x32x16_bf16 v[16:31], a[48:51], v[116:119], v[16:31]\n\t"
      "v_mfma_f32_32x32x16_bf16 v[48:63], a[52:55], v[116:119], v[48:63]\n\t"
      "buffer_load_dwordx4 v[116:119], v137, s[44:47], s91 offen sc1\n\t"
      "s_add_u32 s91, s91, 2048\n\t"
      "s_cmp_eq_u32 s91, s93\n\t"
      "s_cselect_b32 s91, s92, s91\n\t"
      "s_waitcnt vmcnt(15)\n\t"
      "v_mfma_f32_32x32x16_bf16 v[0:15], a[56:59], v[120:123], v[0:15]\n\t"
      "v_mfma_f32_32x32x16_bf16 v[32:47], a[60:63], v[120:123], v[32:47]\n\t"
      "buffer_load_dwordx4 v[120:123], v136, s[44:47], s91 offen sc1\n\t"
      "s_waitcnt vmcnt(15)\n\t"
      "v_mfma_f32_32x32x16_bf16 v[16:31], a[56:59], v[124:127], v[16:31]\n\t"
      "v_mfma_f32_32x32x16_bf16 v[48:63], a[60:63], v[124:127], v[48:63]\n\t"
      "buffer_load_dwordx4 v[124:127], v137, s[44:47], s91 offen sc1\n\t"
      "s_add_u32 s91, s91, 2048\n\t"
      "s_cmp_eq_u32 s91, s93\n\t"
      "s_cselect_b32 s91, s92, s91\n\t"
      "s_waitcnt vmcnt(15)\n\t"
      "v_mfma_f32_32x32x16_bf16 v[0:15], a[64:67], v[64:67], v[0:15]\n\t"
      "v_mfma_f32_32x32x16_bf16 v[32:47], a[68:71], v[64:67], v[32:47]\n\t"
      "buffer_load_dwordx4 v[64:67], v136, s[44:47], s91 offen sc1\n\t"
      "s_waitcnt vmcnt(15)\n\t"
      "v_mfma_f32_32x32x16_bf16 v[16:31], a[64:67], v[68:71], v[16:31]\n\t"
      "v_mfma_f32_32x32x16_bf16 v[48:63], a[68:71], v[68:71], v[48:63]\n\t"
      "buffer_load_dwordx4 v[68:71], v137, s[44:47], s91 offen sc1\n\t"
      "s_add_u32 s91, s91, 2048\n\t"
      "s_cmp_eq_u32 s91, s93\n\t"
      "s_cselect_b32 s91, s92, s91\n\t"
      "s_waitcnt vmcnt(15)\n\t"
      "v_mfma_f32_32x32x16_bf16 v[0:15], a[72:75], v[72:75], v[0:15]\n\t"
      "v_mfma_f32_32x32x16_bf16 v[32:47], a[76:79], v[72:75], v[32:47]\n\t"
      "buffer_load_dwordx4 v[72:75], v136, s[44:47], s91 offen sc1\n\t"
      "s_waitcnt vmcnt(15)\n\t"
      "v_mfma_f32_32x32x16_bf16 v[16:31], a[72:75], v[76:79], v[16:31]\n\t"
      "v_mfma_f32_32x32x16_bf16 v[48:63], a[76:79], v[76:79], v[48:63]\n\t"
      "buffer_load_dwordx4 v[76:79], v137, s[44:47], s91 offen sc1\n\t"
      "s_add_u32 s91, s91, 2048\n\t"
      "s_cmp_eq_u32 s91, s93\n\t"
      "s_cselect_b32 s91, s92, s91\n\t"
      "s_waitcnt vmcnt(15)\n\t"
      "v_mfma_f32_32x32x16_bf16 v[0:15], a[80:83], v[80:83], v[0:15]\n\t"
      "v_mfma_f32_32x32x16_bf16 v[32:47], a[84:87], v[80:83], v[32:47]\n\t"
      "buffer_load_dwordx4 v[80:83], v136, s[44:47], s91 offen sc1\n\t"
      "s_waitcnt vmcnt(15)\n\t"
      "v_mfma_f32_32x32x16_bf16 v[16:31], a[80:83], v[84:87], v[16:31]\n\t"
      "v_mfma_f32_32x32x16_bf16 v[48:63], a[84:87], v[84:87], v[48:63]\n\t"
      "buffer_load_dwordx4 v[84:87], v137, s[44:47], s91 offen sc1\n\t"
      "s_add_u32 s91, s91, 2048\n\t"
      "s_cmp_eq_u32 s91, s93\n\t"
      "s_cselect_b32 s91, s92, s91\n\t"
      "s_waitcnt vmcnt(15)\n\t"
      "v_mfma_f32_32x32x16_bf16 v[0:15], a[88:91], v[88:91], v[0:15]\n\t"
      "v_mfma_f32_32x32x16_bf16 v[32:47], a[92:95], v[88:91], v[32:47]\n\t"
      "buffer_load_dwordx4 v[88:91], v136, s[44:47], s91 offen sc1\n\t"
      "s_waitcnt vmcnt(15)\n\t"
      "v_mfma_f32_32x32x16_bf16 v[16:31], a[88:91], v[92:95], v[16:31]\n\t"
      "v_mfma_f32_32x32x16_bf16 v[48:63], a[92:95], v[92:95], v[48:63]\n\t"
      "buffer_load_dwordx4 v[92:95], v137, s[44:47], s91 offen sc1\n\t"
      "s_add_u32 s91, s91, 2048\n\t"
      "s_cmp_eq_u32 s91, s93\n\t"
      "s_cselect_b32 s91, s92, s91\n\t"
      "s_waitcnt vmcnt(15)\n\t"
      "v_mfma_f32_32x32x16_bf16 v[0:15], a[96:99], v[96:99], v[0:15]\n\t"
      "v_mfma_f32_32x32x16_bf16 v[32:47], a[100:103], v[96:99], v[32:47]\n\t"
      "buffer_load_dwordx4 v[96:99], v136, s[44:47], s91 offen sc1\n\t"
      "s_waitcnt vmcnt(15)\n\t"
      "v_mfma_f32_32x32x16_bf16 v[16:31], a[96:99], v[100:103], v[16:31]\n\t"
      "v_mfma_f32_32x32x16_bf16 v[48:63], a[100:103], v[100:103], v[48:63]\n\t"
      "buffer_load_dwordx4 v[100:103], v137, s[44:47], s91 offen sc1\n\t"
      "s_add_u32 s91, s91, 2048\n\t"
      "s_cmp_eq_u32 s91, s93\n\t"
      "s_cselect_b32 s91, s92, s91\n\t"
      "s_waitcnt vmcnt(15)\n\t"
      "v_mfma_f32_32x32x16_bf16 v[0:15], a[104:107], v[104:107], v[0:15]\n\t"
      "v_mfma_f32_32x32x16_bf16 v[32:47], a[108:111], v[104:107], v[32:47]\n\t"
      "buffer_load_dwordx4 v[104:107], v136, s[44:47], s91 offen sc1\n\t"
      "s_waitcnt vmcnt(15)\n\t"
      "v_mfma_f32_32x32x16_bf16 v[16:31], a[104:107], v[108:111], v[16:31]\n\t"
      "v_mfma_f32_32x32x16_bf16 v[48:63], a[108:111], v[108:111], v[48:63]\n\t"
      "buffer_load_dwordx4 v[108:111], v137, s[44:47], s91 offen sc1\n\t"
      "s_add_u32 s91, s91, 2048\n\t"
      "s_cmp_eq_u32 s91, s93\n\t"
      "s_cselect_b32 s91, s92, s91\n\t"
      "s_waitcnt vmcnt(15)\n\t"
      "v_mfma_f32_32x32x16_bf16 v[0:15], a[112:115], v[112:115], v[0:15]\n\t"
      "v_mfma_f32_32x32x16_bf16 v[32:47], a[116:119], v[112:115], v[32:47]\n\t"
      "buffer_load_dwordx4 v[112:115], v136, s[44:47], s91 offen sc1\n\t"
      "s_waitcnt vmcnt(15)\n\t"
      "v_mfma_f32_32x32x16_bf16 v[16:31], a[112:115], v[116:119], v[16:31]\n\t"
      "v_mfma_f32_32x32x16_bf16 v[48:63], a[116:119], v[116:119], v[48:63]\n\t"
      "buffer_load_dwordx4 v[116:119], v137, s[44:47], s91 offen sc1\n\t"
      "s_add_u32 s91, s91, 2048\n\t"
      "s_cmp_eq_u32 s91, s93\n\t"
      "s_cselect_b32 s91, s92, s91\n\t"
      "s_waitcnt vmcnt(15)\n\t"
      "v_mfma_f32_32x32x16_bf16 v[0:15], a[120:123], v[120:123], v[0:15]\n\t"
      "v_mfma_f32_32x32x16_bf16 v[32:47], a[124:127], v[120:123], v[32:47]\n\t"
      "buffer_load_dwordx4 v[120:123], v136, s[44:47], s91 offen sc1\n\t"
      "s_waitcnt vmcnt(15)\n\t"
      "v_mfma_f32_32x32x16_bf16 v[16:31], a[120:123], v[124:127], v[16:31]\n\t"
      "v_mfma_f32_32x32x16_bf16 v[48:63], a[124:127], v[124:127], v[48:63]\n\t"
      "buffer_load_dwordx4 v[124:127], v137, s[44:47], s91 offen sc1\n\t"
      "s_add_u32 s91, s91, 2048\n\t"
      "s_cmp_eq_u32 s91, s93\n\t"
      "s_cselect_b32 s91, s92, s91\n\t"
      "s_waitcnt vmcnt(15)\n\t"
      "v_mfma_f32_32x32x16_bf16 v[0:15], a[128:131], v[64:67], v[0:15]\n\t"
      "v_mfma_f32_32x32x16_bf16 v[32:47], a[132:135], v[64:67], v[32:47]\n\t"
      "buffer_load_dwordx4 v[64:67], v136, s[44:47], s91 offen sc1\n\t"
      "s_waitcnt vmcnt(15)\n\t"
      "v_mfma_f32_32x32x16_bf16 v[16:31], a[128:131], v[68:71], v[16:31]\n\t"
      "v_mfma_f32_32x32x16_bf16 v[48:63], a[132:135], v[68:71], v[48:63]\n\t"
      "buffer_load_dwordx4 v[68:71], v137, s[44:47], s91 offen sc1\n\t"
      "s_add_u32 s91, s91, 2048\n\t"
      "s_cmp_eq_u32 s91, s93\n\t"
      "s_cselect_b32 s91, s92, s91\n\t"
      "s_waitcnt vmcnt(15)\n\t"
      "v_mfma_f32_32x32x16_bf16 v[0:15], a[136:139], v[72:75], v[0:15]\n\t"
      "v_mfma_f32_32x32x16_bf16 v[32:47], a[140:143], v[72:75], v[32:47]\n\t"
      "buffer_load_dwordx4 v[72:75], v136, s[44:47], s91 offen sc1\n\t"
      "s_waitcnt vmcnt(15)\n\t"
      "v_mfma_f32_32x32x16_bf16 v[16:31], a[136:139], v[76:79], v[16:31]\n\t"
      "v_mfma_f32_32x32x16_bf16 v[48:63], a[140:143], v[76:79], v[48:63]\n\t"
      "buffer_load_dwordx4 v[76:79], v137, s[44:47], s91 offen sc1\n\t"
      "s_add_u32 s91, s91, 2048\n\t"
      "s_cmp_eq_u32 s91, s93\n\t"
      "s_cselect_b32 s91, s92, s91\n\t"
      "s_waitcnt vmcnt(15)\n\t"
      "v_mfma_f32_32x32x16_bf16 v[0:15], a[144:147], v[80:83], v[0:15]\n\t"
      "v_mfma_f32_32x32x16_bf16 v[32:47], a[148:151], v[80:83], v[32:47]\n\t"
      "buffer_load_dwordx4 v[80:83], v136, s[44:47], s91 offen sc1\n\t"
      "s_waitcnt vmcnt(15)\n\t"
      "v_mfma_f32_32x32x16_bf16 v[16:31], a[144:147], v[84:87], v[16:31]\n\t"
      "v_mfma_f32_32x32x16_bf16 v[48:63], a[148:151], v[84:87], v[48:63]\n\t"
      "buffer_load_dwordx4 v[84:87], v137, s[44:47], s91 offen sc1\n\t"
      "s_add_u32 s91, s91, 2048\n\t"
      "s_cmp_eq_u32 s91, s93\n\t"
      "s_cselect_b32 s91, s92, s91\n\t"
      "s_waitcnt vmcnt(15)\n\t"
      "v_mfma_f32_32x32x16_bf16 v[0:15], a[152:155], v[88:91], v[0:15]\n\t"
      "v_mfma_f32_32x32x16_bf16 v[32:47], a[156:159], v[88:91], v[32:47]\n\t"
      "buffer_load_dwordx4 v[88:91], v136, s[44:47], s91 offen sc1\n\t"
      "s_waitcnt vmcnt(15)\n\t"
      "v_mfma_f32_32x32x16_bf16 v[16:31], a[152:155], v[92:95], v[16:31]\n\t"
      "v_mfma_f32_32x32x16_bf16 v[48:63], a[156:159], v[92:95], v[48:63]\n\t"
      "buffer_load_dwordx4 v[92:95], v137, s[44:47], s91 offen sc1\n\t"
      "s_add_u32 s91, s91, 2048\n\t"
      "s_cmp_eq_u32 s91, s93\n\t"
      "s_cselect_b32 s91, s92, s91\n\t"
      "s_waitcnt vmcnt(15)\n\t"
      "v_mfma_f32_32x32x16_bf16 v[0:15], a[160:163], v[96:99], v[0:15]\n\t"
      "v_mfma_f32_32x32x16_bf16 v[32:47], a[164:167], v[96:99], v[32:47]\n\t"
      "buffer_load_dwordx4 v[96:99], v136, s[44:47], s91 offen sc1\n\t"
      "s_waitcnt vmcnt(15)\n\t"
      "v_mfma_f32_32x32x16_bf16 v[16:31], a[160:163], v[100:103], v[16:31]\n\t"
      "v_mfma_f32_32x32x16_bf16 v[48:63], a[164:167], v[100:103], v[48:63]\n\t"
      "buffer_load_dwordx4 v[100:103], v137, s[44:47], s91 offen sc1\n\t"
      "s_add_u32 s91, s91, 2048\n\t"
      "s_cmp_eq_u32 s91, s93\n\t"
      "s_cselect_b32 s91, s92, s91\n\t"
      "s_waitcnt vmcnt(15)\n\t"
      "v_mfma_f32_32x32x16_bf16 v[0:15], a[168:171], v[104:107], v[0:15]\n\t"
      "v_mfma_f32_32x32x16_bf16 v[32:47], a[172:175], v[104:107], v[32:47]\n\t"
      "buffer_load_dwordx4 v[104:107], v136, s[44:47], s91 offen sc1\n\t"
      "s_waitcnt vmcnt(15)\n\t"
      "v_mfma_f32_32x32x16_bf16 v[16:31], a[168:171], v[108:111], v[16:31]\n\t"
      "v_mfma_f32_32x32x16_bf16 v[48:63], a[172:175], v[108:111], v[48:63]\n\t"
      "buffer_load_dwordx4 v[108:111], v137, s[44:47], s91 offen sc1\n\t"
      "s_add_u32 s91, s91, 2048\n\t"
      "s_cmp_eq_u32 s91, s93\n\t"
      "s_cselect_b32 s91, s92, s91\n\t"
      "s_waitcnt vmcnt(15)\n\t"
      "v_mfma_f32_32x32x16_bf16 v[0:15], a[176:179], v[112:115], v[0:15]\n\t"
      "v_mfma_f32_32x32x16_bf16 v[32:47], a[180:183], v[112:115], v[32:47]\n\t"
      "buffer_load_dwordx4 v[112:115], v136, s[44:47], s91 offen sc1\n\t"
      "s_waitcnt vmcnt(15)\n\t"
      "v_mfma_f32_32x32x16_bf16 v[16:31], a[176:179], v[116:119], v[16:31]\n\t"
      "v_mfma_f32_32x32x16_bf16 v[48:63], a[180:183], v[116:119], v[48:63]\n\t"
      "buffer_load_dwordx4 v[116:119], v137, s[44:47], s91 offen sc1\n\t"
      "s_add_u32 s91, s91, 2048\n\t"
      "s_cmp_eq_u32 s91, s93\n\t"
      "s_cselect_b32 s91, s92, s91\n\t"
      "s_waitcnt vmcnt(15)\n\t"
      "v_mfma_f32_32x32x16_bf16 v[0:15], a[184:187], v[120:123], v[0:15]\n\t"
      "v_mfma_f32_32x32x16_bf16 v[32:47], a[188:191], v[120:123], v[32:47]\n\t"
      "buffer_load_dwordx4 v[120:123], v136, s[44:47], s91 offen sc1\n\t"
      "s_waitcnt vmcnt(15)\n\t"
      "v_mfma_f32_32x32x16_bf16 v[16:31], a[184:187], v[124:127], v[16:31]\n\t"
      "v_mfma_f32_32x32x16_bf16 v[48:63], a[188:191], v[124:127], v[48:63]\n\t"
      "buffer_load_dwordx4 v[124:127], v137, s[44:47], s91 offen sc1\n\t"
      "s_add_u32 s91, s91, 2048\n\t"
      "s_cmp_eq_u32 s91, s93\n\t"
      "s_cselect_b32 s91, s92, s91\n\t"
      "s_waitcnt vmcnt(15)\n\t"
      "v_mfma_f32_32x32x16_bf16 v[0:15], a[192:195], v[64:67], v[0:15]\n\t"
      "v_mfma_f32_32x32x16_bf16 v[32:47], a[196:199], v[64:67], v[32:47]\n\t"
      "s_waitcnt vmcnt(14)\n\t"
      "v_mfma_f32_32x32x16_bf16 v[16:31], a[192:195], v[68:71], v[16:31]\n\t"
      "v_mfma_f32_32x32x16_bf16 v[48:63], a[196:199], v[68:71], v[48:63]\n\t"
      "s_waitcnt vmcnt(13)\n\t"
      "v_mfma_f32_32x32x16_bf16 v[0:15], a[200:203], v[72:75], v[0:15]\n\t"
      "v_mfma_f32_32x32x16_bf16 v[32:47], a[204:207], v[72:75], v[32:47]\n\t"
      "s_waitcnt vmcnt(12)\n\t"
      "v_mfma_f32_32x32x16_bf16 v[16:31], a[200:203], v[76:79], v[16:31]\n\t"
      "v_mfma_f32_32x32x16_bf16 v[48:63], a[204:207], v[76:79], v[48:63]\n\t"
      "s_waitcnt vmcnt(11)\n\t"
      "v_mfma_f32_32x32x16_bf16 v[0:15], a[208:211], v[80:83], v[0:15]\n\t"
      "v_mfma_f32_32x32x16_bf16 v[32:47], a[212:215], v[80:83], v[32:47]\n\t"
      "s_waitcnt vmcnt(10)\n\t"
      "v_mfma_f32_32x32x16_bf16 v[16:31], a[208:211], v[84:87], v[16:31]\n\t"
      "v_mfma_f32_32x32x16_bf16 v[48:63], a[212:215], v[84:87], v[48:63]\n\t"
      "s_add_u32 m0, s70, 0\n\t"
      "s_add_u32 s99, s67, 65536\n\t"
      "buffer_load_dwordx4 v132, s[40:43], s67 offen lds\n\t"
      "buffer_load_dwordx4 v133, s[40:43], s67 offen offset:1024 lds\n\t"
      "s_cmp_eq_u32 s99, s69\n\t"
      "s_cselect_b32 s67, s68, s99\n\t"
      "s_waitcnt vmcnt(11)\n\t"
      "v_mfma_f32_32x32x16_bf16 v[0:15], a[216:219], v[88:91], v[0:15]\n\t"
      "v_mfma_f32_32x32x16_bf16 v[32:47], a[220:223], v[88:91], v[32:47]\n\t"
      "s_waitcnt vmcnt(10)\n\t"
      "v_mfma_f32_32x32x16_bf16 v[16:31], a[216:219], v[92:95], v[16:31]\n\t"
      "v_mfma_f32_32x32x16_bf16 v[48:63], a[220:223], v[92:95], v[48:63]\n\t"
      "s_add_u32 m0, s70, 2048\n\t"
      "s_add_u32 s99, s67, 65536\n\t"
      "buffer_load_dwordx4 v132, s[40:43], s67 offen lds\n\t"
      "buffer_load_dwordx4 v133, s[40:43], s67 offen offset:1024 lds\n\t"
      "s_cmp_eq_u32 s99, s69\n\t"
      "s_cselect_b32 s67, s68, s99\n\t"
      "s_waitcnt vmcnt(11)\n\t"
      "v_mfma_f32_32x32x16_bf16 v[0:15], a[224:227], v[96:99], v[0:15]\n\t"
      "v_mfma_f32_32x32x16_bf16 v[32:47], a[228:231], v[96:99], v[32:47]\n\t"
      "s_waitcnt vmcnt(10)\n\t"
      "v_mfma_f32_32x32x16_bf16 v[16:31], a[224:227], v[100:103], v[16:31]\n\t"
      "v_mfma_f32_32x32x16_bf16 v[48:63], a[228:231], v[100:103], v[48:63]\n\t"
      "s_add_u32 m0, s70, 4096\n\t"
      "s_add_u32 s99, s67, 65536\n\t"
      "buffer_load_dwordx4 v132, s[40:43], s67 offen lds\n\t"
      "buffer_load_dwordx4 v133, s[40:43], s67 offen offset:1024 lds\n\t"
      "s_cmp_eq_u32 s99, s69\n\t"
      "s_cselect_b32 s67, s68, s99\n\t"
      "s_waitcnt vmcnt(11)\n\t"
      "v_mfma_f32_32x32x16_bf16 v[0:15], a[232:235], v[104:107], v[0:15]\n\t"
      "v_mfma_f32_32x32x16_bf16 v[32:47], a[236:239], v[104:107], v[32:47]\n\t"
      "s_waitcnt vmcnt(10)\n\t"
      "v_mfma_f32_32x32x16_bf16 v[16:31], a[232:235], v[108:111], v[16:31]\n\t"
      "v_mfma_f32_32x32x16_bf16 v[48:63], a[236:239], v[108:111], v[48:63]\n\t"
      "s_add_u32 m0, s70, 6144\n\t"
      "s_add_u32 s99, s67, 65536\n\t"
      "buffer_load_dwordx4 v132, s[40:43], s67 offen lds\n\t"
      "buffer_load_dwordx4 v133, s[40:43], s67 offen offset:1024 lds\n\t"
      "s_cmp_eq_u32 s99, s69\n\t"
      "s_cselect_b32 s67, s68, s99\n\t"
      "s_waitcnt vmcnt(11)\n\t"
      "v_mfma_f32_32x32x16_bf16 v[0:15], a[240:243], v[112:115], v[0:15]\n\t"
      "v_mfma_f32_32x32x16_bf16 v[32:47], a[244:247], v[112:115], v[32:47]\n\t"
      "s_waitcnt vmcnt(10)\n\t"
      "v_mfma_f32_32x32x16_bf16 v[16:31], a[240:243], v[116:119], v[16:31]\n\t"
      "v_mfma_f32_32x32x16_bf16 v[48:63], a[244:247], v[116:119], v[48:63]\n\t"
      "s_add_u32 m0, s70, 8192\n\t"
      "s_add_u32 s99, s67, 65536\n\t"
      "buffer_load_dwordx4 v132, s[40:43], s67 offen lds\n\t"
      "buffer_load_dwordx4 v133, s[40:43], s67 offen offset:1024 lds\n\t"
      "s_cmp_eq_u32 s99, s69\n\t"
      "s_cselect_b32 s67, s68, s99\n\t"
      "s_waitcnt vmcnt(11)\n\t"
      "v_mfma_f32_32x32x16_bf16 v[0:15], a[248:251], v[120:123], v[0:15]\n\t"
      "v_mfma_f32_32x32x16_bf16 v[32:47], a[252:255], v[120:123], v[32:47]\n\t"
      "s_waitcnt vmcnt(10)\n\t"
      "v_mfma_f32_32x32x16_bf16 v[16:31], a[248:251], v[124:127], v[16:31]\n\t"
      "v_mfma_f32_32x32x16_bf16 v[48:63], a[252:255], v[124:127], v[48:63]\n\t"
      "s_add_u32 m0, s70, 10240\n\t"
      "s_add_u32 s99, s67, 65536\n\t"
      "buffer_load_dwordx4 v132, s[40:43], s67 offen lds\n\t"
      "buffer_load_dwordx4 v133, s[40:43], s67 offen offset:1024 lds\n\t"
      "s_cmp_eq_u32 s99, s69\n\t"
      "s_cselect_b32 s67, s68, s99\n\t"
      "s_nop 7\n\t"
      "s_nop 3\n\t"
      "ds_write_b128 v150, v[32:35] offset:0\n\t"
      "ds_write_b128 v150, v[36:39] offset:1024\n\t"
      "ds_write_b128 v150, v[40:43] offset:2048\n\t"
      "ds_write_b128 v150, v[44:47] offset:3072\n\t"
      "s_add_u32 m0, s70, 12288\n\t"
      "s_add_u32 s99, s67, 65536\n\t"
      "buffer_load_dwordx4 v132, s[40:43], s67 offen lds\n\t"
      "buffer_load_dwordx4 v133, s[40:43], s67 offen offset:1024 lds\n\t"
      "s_cmp_eq_u32 s99, s69\n\t"
      "s_cselect_b32 s67, s68, s99\n\t"
      "ds_write_b128 v149, v[16:19] offset:0\n\t"
      "ds_write_b128 v149, v[20:23] offset:1024\n\t"
      "ds_write_b128 v149, v[24:27] offset:2048\n\t"
      "ds_write_b128 v149, v[28:31] offset:3072\n\t"
      "s_add_u32 m0, s70, 14336\n\t"
      "s_add_u32 s99, s67, 65536\n\t"
      "buffer_load_dwordx4 v132, s[40:43], s67 offen lds\n\t"
      "buffer_load_dwordx4 v133, s[40:43], s67 offen offset:1024 lds\n\t"
      "s_cmp_eq_u32 s99, s69\n\t"
      "s_cselect_b32 s67, s68, s99\n\t"
      "ds_write_b128 v151, v[48:51] offset:0\n\t"
      "ds_write_b128 v151, v[52:55] offset:1024\n\t"
      "ds_write_b128 v151, v[56:59] offset:2048\n\t"
      "ds_write_b128 v151, v[60:63] offset:3072\n\t"
      "s_add_u32 m0, s70, 16384\n\t"
      "s_add_u32 s99, s67, 65536\n\t"
      "buffer_load_dwordx4 v132, s[40:43], s67 offen lds\n\t"
      "buffer_load_dwordx4 v133, s[40:43], s67 offen offset:1024 lds\n\t"
      "s_cmp_eq_u32 s99, s69\n\t"
      "s_cselect_b32 s67, s68, s99\n\t"
      "s_waitcnt lgkmcnt(0)\n\t"
      "s_barrier\n\t"
      "ds_read_b128 v[64:67], v152 offset:0\n\t"
      "ds_read_b128 v[68:71], v152 offset:1024\n\t"
      "ds_read_b128 v[72:75], v152 offset:2048\n\t"
      "ds_read_b128 v[76:79], v152 offset:3072\n\t"
      "s_add_u32 m0, s70, 18432\n\t"
      "s_add_u32 s99, s67, 65536\n\t"
      "buffer_load_dwordx4 v132, s[40:43], s67 offen lds\n\t"
      "buffer_load_dwordx4 v133, s[40:43], s67 offen offset:1024 lds\n\t"
      "s_cmp_eq_u32 s99, s69\n\t"
      "s_cselect_b32 s67, s68, s99\n\t"
      "ds_read_b128 v[80:83], v152 offset:4096\n\t"
      "ds_read_b128 v[84:87], v152 offset:5120\n\t"
      "ds_read_b128 v[88:91], v152 offset:6144\n\t"
      "ds_read_b128 v[92:95], v152 offset:7168\n\t"
      "s_add_u32 m0, s70, 20480\n\t"
      "s_add_u32 s99, s67, 65536\n\t"
      "buffer_load_dwordx4 v132, s[40:43], s67 offen lds\n\t"
      "buffer_load_dwordx4 v133, s[40:43], s67 offen offset:1024 lds\n\t"
      "s_cmp_eq_u32 s99, s69\n\t"
      "s_cselect_b32 s67, s68, s99\n\t"
      "ds_read_b128 v[96:99], v152 offset:8192\n\t"
      "ds_read_b128 v[100:103], v152 offset:9216\n\t"
      "ds_read_b128 v[104:107], v152 offset:10240\n\t"
      "ds_read_b128 v[108:111], v152 offset:11264\n\t"
      "s_add_u32 m0, s70, 22528\n\t"
      "s_add_u32 s99, s67, 65536\n\t"
      "buffer_load_dwordx4 v132, s[40:43], s67 offen lds\n\t"
      "buffer_load_dwordx4 v133, s[40:43], s67 offen offset:1024 lds\n\t"
      "s_cmp_eq_u32 s99, s69\n\t"
      "s_cselect_b32 s67, s68, s99\n\t"
      "s_waitcnt lgkmcnt(8)\n\t"
      "v_pk_add_f32 v[0:1], v[0:1], v[64:65]\n\t"
      "v_pk_add_f32 v[2:3], v[2:3], v[66:67]\n\t"
      "v_pk_add_f32 v[4:5], v[4:5], v[68:69]\n\t"
      "v_pk_add_f32 v[6:7], v[6:7], v[70:71]\n\t"
      "v_pk_add_f32 v[8:9], v[8:9], v[72:73]\n\t"
      "v_pk_add_f32 v[10:11], v[10:11], v[74:75]\n\t"
      "v_pk_add_f32 v[12:13], v[12:13], v[76:77]\n\t"
      "v_pk_add_f32 v[14:15], v[14:15], v[78:79]\n\t"
      "s_waitcnt vmcnt(22)\n\t"
      "ds_read_b64_tr_b16 a[0:1], v134 offset:0\n\t"
      "ds_read_b64_tr_b16 a[2:3], v134 offset:512\n\t"
      "ds_read_b64_tr_b16 a[4:5], v135 offset:0\n\t"
      "ds_read_b64_tr_b16 a[6:7], v135 offset:512\n\t"
      "s_add_u32 m0, s70, 24576\n\t"
      "s_add_u32 s99, s67, 65536\n\t"
      "buffer_load_dwordx4 v132, s[40:43], s67 offen lds\n\t"
      "buffer_load_dwordx4 v133, s[40:43], s67 offen offset:1024 lds\n\t"
      "s_cmp_eq_u32 s99, s69\n\t"
      "s_cselect_b32 s67, s68, s99\n\t"
      "s_waitcnt lgkmcnt(8)\n\t"
      "v_pk_add_f32 v[0:1], v[0:1], v[80:81]\n\t"
      "v_pk_add_f32 v[2:3], v[2:3], v[82:83]\n\t"
      "v_pk_add_f32 v[4:5], v[4:5], v[84:85]\n\t"
      "v_pk_add_f32 v[6:7], v[6:7], v[86:87]\n\t"
      "v_pk_add_f32 v[8:9], v[8:9], v[88:89]\n\t"
      "v_pk_add_f32 v[10:11], v[10:11], v[90:91]\n\t"
      "v_pk_add_f32 v[12:13], v[12:13], v[92:93]\n\t"
      "v_pk_add_f32 v[14:15], v[14:15], v[94:95]\n\t"
      "s_waitcnt vmcnt(22)\n\t"
      "ds_read_b64_tr_b16 a[8:9], v134 offset:2048\n\t"
      "ds_read_b64_tr_b16 a[10:11], v134 offset:2560\n\t"
      "ds_read_b64_tr_b16 a[12:13], v135 offset:2048\n\t"
      "ds_read_b64_tr_b16 a[14:15], v135 offset:2560\n\t"
      "s_add_u32 m0, s70, 26624\n\t"
      "s_add_u32 s99, s67, 65536\n\t"
      "buffer_load_dwordx4 v132, s[40:43], s67 offen lds\n\t"
      "buffer_load_dwordx4 v133, s[40:43], s67 offen offset:1024 lds\n\t"
      "s_cmp_eq_u32 s99, s69\n\t"
      "s_cselect_b32 s67, s68, s99\n\t"
      "s_waitcnt lgkmcnt(8)\n\t"
      "v_pk_add_f32 v[0:1], v[0:1], v[96:97]\n\t"
      "v_pk_add_f32 v[2:3], v[2:3], v[98:99]\n\t"
      "v_pk_add_f32 v[4:5], v[4:5], v[100:101]\n\t"
      "v_pk_add_f32 v[6:7], v[6:7], v[102:103]\n\t"
      "v_pk_add_f32 v[8:9], v[8:9], v[104:105]\n\t"
      "v_pk_add_f32 v[10:11], v[10:11], v[106:107]\n\t"
      "v_pk_add_f32 v[12:13], v[12:13], v[108:109]\n\t"
      "v_pk_add_f32 v[14:15], v[14:15], v[110:111]\n\t"
      "s_waitcnt vmcnt(22)\n\t"
      "ds_read_b64_tr_b16 a[16:17], v134 offset:4096\n\t"
      "ds_read_b64_tr_b16 a[18:19], v134 offset:4608\n\t"
      "ds_read_b64_tr_b16 a[20:21], v135 offset:4096\n\t"
      "ds_read_b64_tr_b16 a[22:23], v135 offset:4608\n\t"
      "s_waitcnt lgkmcnt(8)\n\t"
      "s_add_u32 m0, s70, 0\n\t"
      "s_add_u32 s99, s67, 65536\n\t"
      "buffer_load_dwordx4 v132, s[40:43], s67 offen lds\n\t"
      "buffer_load_dwordx4 v133, s[40:43], s67 offen offset:1024 lds\n\t"
      "s_cmp_eq_u32 s99, s69\n\t"
      "s_cselect_b32 s67, s68, s99\n\t"
      "v_pk_add_f32 v[0:1], v[0:1], v[176:177]\n\t"
      "v_pk_add_f32 v[2:3], v[2:3], v[178:179]\n\t"
      "v_pk_add_f32 v[4:5], v[4:5], v[180:181]\n\t"
      "v_pk_add_f32 v[6:7], v[6:7], v[182:183]\n\t"
      "v_pk_add_f32 v[8:9], v[8:9], v[184:185]\n\t"
      "v_pk_add_f32 v[10:11], v[10:11], v[186:187]\n\t"
      "v_pk_add_f32 v[12:13], v[12:13], v[188:189]\n\t"
      "v_pk_add_f32 v[14:15], v[14:15], v[190:191]\n\t"
      "s_waitcnt vmcnt(22)\n\t"
      "ds_read_b64_tr_b16 a[24:25], v134 offset:6144\n\t"
      "ds_read_b64_tr_b16 a[26:27], v134 offset:6656\n\t"
      "ds_read_b64_tr_b16 a[28:29], v135 offset:6144\n\t"
      "ds_read_b64_tr_b16 a[30:31], v135 offset:6656\n\t"
      "s_waitcnt lgkmcnt(8)\n\t"
      "s_add_u32 m0, s70, 2048\n\t"
      "s_add_u32 s99, s67, 65536\n\t"
      "buffer_load_dwordx4 v132, s[40:43], s67 offen lds\n\t"
      "buffer_load_dwordx4 v133, s[40:43], s67 offen offset:1024 lds\n\t"
      "s_cmp_eq_u32 s99, s69\n\t"
      "s_cselect_b32 s67, s68, s99\n\t"
      "v_pk_mul_f32 v[16:17], v[0:1], v[0:1]\n\t"
      "v_pk_mul_f32 v[18:19], v[2:3], v[2:3]\n\t"
      "v_pk_mul_f32 v[20:21], v[4:5], v[4:5]\n\t"
      "v_pk_mul_f32 v[22:23], v[6:7], v[6:7]\n\t"
      "v_pk_mul_f32 v[24:25], v[8:9], v[8:9]\n\t"
      "v_pk_mul_f32 v[26:27], v[10:11], v[10:11]\n\t"
      "v_pk_mul_f32 v[28:29], v[12:13], v[12:13]\n\t"
      "v_pk_mul_f32 v[30:31], v[14:15], v[14:15]\n\t"
      "v_pk_fma_f32 v[16:17], v[16:17], v[206:207], v[204:205]\n\t"
      "v_pk_fma_f32 v[18:19], v[18:19], v[206:207], v[204:205]\n\t"
      "v_pk_fma_f32 v[20:21], v[20:21], v[206:207], v[204:205]\n\t"
      "v_pk_fma_f32 v[22:23], v[22:23], v[206:207], v[204:205]\n\t"
      "v_pk_fma_f32 v[24:25], v[24:25], v[206:207], v[204:205]\n\t"
      "v_pk_fma_f32 v[26:27], v[26:27], v[206:207], v[204:205]\n\t"
      "v_pk_fma_f32 v[28:29], v[28:29], v[206:207], v[204:205]\n\t"
      "v_pk_fma_f32 v[30:31], v[30:31], v[206:207], v[204:205]\n\t"
      "s_waitcnt vmcnt(22)\n\t"
      "ds_read_b64_tr_b16 a[32:33], v134 offset:8192\n\t"
      "ds_read_b64_tr_b16 a[34:35], v134 offset:8704\n\t"
      "ds_read_b64_tr_b16 a[36:37], v135 offset:8192\n\t"
      "ds_read_b64_tr_b16 a[38:39], v135 offset:8704\n\t"
      "s_waitcnt lgkmcnt(8)\n\t"
      "s_add_u32 m0, s70, 4096\n\t"
      "s_add_u32 s99, s67, 65536\n\t"
      "buffer_load_dwordx4 v132, s[40:43], s67 offen lds\n\t"
      "buffer_load_dwordx4 v133, s[40:43], s67 offen offset:1024 lds\n\t"
      "s_cmp_eq_u32 s99, s69\n\t"
      "s_cselect_b32 s67, s68, s99\n\t"
      "v_pk_mul_f32 v[16:17], v[16:17], v[0:1]\n\t"
      "v_pk_mul_f32 v[18:19], v[18:19], v[2:3]\n\t"
      "v_pk_mul_f32 v[20:21], v[20:21], v[4:5]\n\t"
      "v_pk_mul_f32 v[22:23], v[22:23], v[6:7]\n\t"
      "v_pk_mul_f32 v[24:25], v[24:25], v[8:9]\n\t"
      "v_pk_mul_f32 v[26:27], v[26:27], v[10:11]\n\t"
      "v_pk_mul_f32 v[28:29], v[28:29], v[12:13]\n\t"
      "v_pk_mul_f32 v[30:31], v[30:31], v[14:15]\n\t"
      "v_exp_f32 v16, v16\n\t"
      "v_exp_f32 v17, v17\n\t"
      "v_exp_f32 v18, v18\n\t"
      "v_exp_f32 v19, v19\n\t"
      "v_exp_f32 v20, v20\n\t"
      "v_exp_f32 v21, v21\n\t"
      "v_exp_f32 v22, v22\n\t"
      "v_exp_f32 v23, v23\n\t"
      "s_waitcnt vmcnt(22)\n\t"
      "ds_read_b64_tr_b16 a[40:41], v134 offset:10240\n\t"
      "ds_read_b64_tr_b16 a[42:43], v134 offset:10752\n\t"
      "ds_read_b64_tr_b16 a[44:45], v135 offset:10240\n\t"
      "ds_read_b64_tr_b16 a[46:47], v135 offset:10752\n\t"
      "s_waitcnt lgkmcnt(8)\n\t"
      "s_add_u32 m0, s70, 6144\n\t"
      "s_add_u32 s99, s67, 65536\n\t"
      "buffer_load_dwordx4 v132, s[40:43], s67 offen lds\n\t"
      "buffer_load_dwordx4 v133, s[40:43], s67 offen offset:1024 lds\n\t"
      "s_cmp_eq_u32 s99, s69\n\t"
      "s_cselect_b32 s67, s68, s99\n\t"
      "v_exp_f32 v24, v24\n\t"
      "v_exp_f32 v25, v25\n\t"
      "v_exp_f32 v26, v26\n\t"
      "v_exp_f32 v27, v27\n\t"
      "v_exp_f32 v28, v28\n\t"
      "v_exp_f32 v29, v29\n\t"
      "v_exp_f32 v30, v30\n\t"
      "v_exp_f32 v31, v31\n\t"
      "s_waitcnt vmcnt(22)\n\t"
      "ds_read_b64_tr_b16 a[48:49], v134 offset:12288\n\t"
      "ds_read_b64_tr_b16 a[50:51], v134 offset:12800\n\t"
      "ds_read_b64_tr_b16 a[52:53], v135 offset:12288\n\t"
      "ds_read_b64_tr_b16 a[54:55], v135 offset:12800\n\t"
      "s_waitcnt lgkmcnt(8)\n\t"
      "s_add_u32 m0, s70, 8192\n\t"
      "s_add_u32 s99, s67, 65536\n\t"
      "buffer_load_dwordx4 v132, s[40:43], s67 offen lds\n\t"
      "buffer_load_dwordx4 v133, s[40:43], s67 offen offset:1024 lds\n\t"
      "s_cmp_eq_u32 s99, s69\n\t"
      "s_cselect_b32 s67, s68, s99\n\t"
      "v_pk_add_f32 v[16:17], v[16:17], v[208:209]\n\t"
      "v_pk_add_f32 v[18:19], v[18:19], v[208:209]\n\t"
      "v_pk_add_f32 v[20:21], v[20:21], v[208:209]\n\t"
      "v_pk_add_f32 v[22:23], v[22:23], v[208:209]\n\t"
      "v_pk_add_f32 v[24:25], v[24:25], v[208:209]\n\t"
      "v_pk_add_f32 v[26:27], v[26:27], v[208:209]\n\t"
      "v_pk_add_f32 v[28:29], v[28:29], v[208:209]\n\t"
      "v_pk_add_f32 v[30:31], v[30:31], v[208:209]\n\t"
      "v_rcp_f32 v16, v16\n\t"
      "v_rcp_f32 v17, v17\n\t"
      "v_rcp_f32 v18, v18\n\t"
      "v_rcp_f32 v19, v19\n\t"
      "v_rcp_f32 v20, v20\n\t"
      "v_rcp_f32 v21, v21\n\t"
      "v_rcp_f32 v22, v22\n\t"
      "v_rcp_f32 v23, v23\n\t"
      "s_waitcnt vmcnt(22)\n\t"
      "ds_read_b64_tr_b16 a[56:57], v134 offset:14336\n\t"
      "ds_read_b64_tr_b16 a[58:59], v134 offset:14848\n\t"
      "ds_read_b64_tr_b16 a[60:61], v135 offset:14336\n\t"
      "ds_read_b64_tr_b16 a[62:63], v135 offset:14848\n\t"
      "s_waitcnt lgkmcnt(8)\n\t"
      "s_add_u32 m0, s70, 10240\n\t"
      "s_add_u32 s99, s67, 65536\n\t"
      "buffer_load_dwordx4 v132, s[40:43], s67 offen lds\n\t"
      "buffer_load_dwordx4 v133, s[40:43], s67 offen offset:1024 lds\n\t"
      "s_cmp_eq_u32 s99, s69\n\t"
      "s_cselect_b32 s67, s68, s99\n\t"
      "v_rcp_f32 v24, v24\n\t"
      "v_rcp_f32 v25, v25\n\t"
      "v_rcp_f32 v26, v26\n\t"
      "v_rcp_f32 v27, v27\n\t"
      "v_rcp_f32 v28, v28\n\t"
      "v_rcp_f32 v29, v29\n\t"
      "v_rcp_f32 v30, v30\n\t"
      "v_rcp_f32 v31, v31\n\t"
      "s_nop 0\n\t"
      "v_pk_mul_f32 v[0:1], v[0:1], v[16:17]\n\t"
      "v_pk_mul_f32 v[2:3], v[2:3], v[18:19]\n\t"
      "v_pk_mul_f32 v[4:5], v[4:5], v[20:21]\n\t"
      "v_pk_mul_f32 v[6:7], v[6:7], v[22:23]\n\t"
      "v_pk_mul_f32 v[8:9], v[8:9], v[24:25]\n\t"
      "v_pk_mul_f32 v[10:11], v[10:11], v[26:27]\n\t"
      "v_pk_mul_f32 v[12:13], v[12:13], v[28:29]\n\t"
      "v_pk_mul_f32 v[14:15], v[14:15], v[30:31]\n\t"
      "v_cvt_pk_bf16_f32 v32, v0, v1\n\t"
      "v_cvt_pk_bf16_f32 v33, v2, v3\n\t"
      "v_cvt_pk_bf16_f32 v34, v4, v5\n\t"
      "v_cvt_pk_bf16_f32 v35, v6, v7\n\t"
      "v_cvt_pk_bf16_f32 v36, v8, v9\n\t"
      "v_cvt_pk_bf16_f32 v37, v10, v11\n\t"
      "v_cvt_pk_bf16_f32 v38, v12, v13\n\t"
      "v_cvt_pk_bf16_f32 v39, v14, v15\n\t"
      "buffer_store_dwordx4 v[32:35], v154, s[48:51], s71 offen sc1\n\t"
      "buffer_store_dwordx4 v[36:39], v154, s[48:51], s71 offen offset:2048 sc1\n\t"
      "s_waitcnt lgkmcnt(4)\n\t"
      "s_add_u32 m0, s70, 12288\n\t"
      "s_add_u32 s99, s67, 65536\n\t"
      "buffer_load_dwordx4 v132, s[40:43], s67 offen lds\n\t"
      "buffer_load_dwordx4 v133, s[40:43], s67 offen offset:1024 lds\n\t"
      "s_cmp_eq_u32 s99, s69\n\t"
      "s_cselect_b32 s67, s68, s99\n\t"
      "s_waitcnt vmcnt(26)\n\t"
      "ds_read_b64_tr_b16 a[64:65], v134 offset:16384\n\t"
      "ds_read_b64_tr_b16 a[66:67], v134 offset:16896\n\t"
      "ds_read_b64_tr_b16 a[68:69], v135 offset:16384\n\t"
      "ds_read_b64_tr_b16 a[70:71], v135 offset:16896\n\t"
      "s_waitcnt lgkmcnt(4)\n\t"
      "s_add_u32 m0, s70, 14336\n\t"
      "s_add_u32 s99, s67, 65536\n\t"
      "buffer_load_dwordx4 v132, s[40:43], s67 offen lds\n\t"
      "buffer_load_dwordx4 v133, s[40:43], s67 offen offset:1024 lds\n\t"
      "s_cmp_eq_u32 s99, s69\n\t"
      "s_cselect_b32 s67, s68, s99\n\t"
      "s_waitcnt vmcnt(26)\n\t"
      "ds_read_b64_tr_b16 a[72:73], v134 offset:18432\n\t"
      "ds_read_b64_tr_b16 a[74:75], v134 offset:18944\n\t"
      "ds_read_b64_tr_b16 a[76:77], v135 offset:18432\n\t"
      "ds_read_b64_tr_b16 a[78:79], v135 offset:18944\n\t"
      "s_waitcnt lgkmcnt(4)\n\t"
      "s_add_u32 m0, s70, 16384\n\t"
      "s_add_u32 s99, s67, 65536\n\t"
      "buffer_load_dwordx4 v132, s[40:43], s67 offen lds\n\t"
      "buffer_load_dwordx4 v133, s[40:43], s67 offen offset:1024 lds\n\t"
      "s_cmp_eq_u32 s99, s69\n\t"
      "s_cselect_b32 s67, s68, s99\n\t"
      "s_waitcnt vmcnt(26)\n\t"
      "ds_read_b64_tr_b16 a[80:81], v134 offset:20480\n\t"
      "ds_read_b64_tr_b16 a[82:83], v134 offset:20992\n\t"
      "ds_read_b64_tr_b16 a[84:85], v135 offset:20480\n\t"
      "ds_read_b64_tr_b16 a[86:87], v135 offset:20992\n\t"
      "s_waitcnt vmcnt(24)\n\t"
      "ds_read_b64_tr_b16 a[88:89], v134 offset:22528\n\t"
      "ds_read_b64_tr_b16 a[90:91], v134 offset:23040\n\t"
      "ds_read_b64_tr_b16 a[92:93], v135 offset:22528\n\t"
      "ds_read_b64_tr_b16 a[94:95], v135 offset:23040\n\t"
      "s_waitcnt vmcnt(6)\n\t"
      "s_barrier\n\t"
      "s_cmp_lg_u32 s56, 0\n\t"
      "s_cbranch_scc1 Lnofl6_%=\n\t"
      "s_mov_b64 exec, 1\n\t"
      "global_store_dwordx2 v153, v[138:139], s[34:35] sc1\n\t"
      "s_cmp_lg_u32 s57, 0\n\t"
      "s_cbranch_scc1 Lnoe7_%=\n\t"
      "v_mov_b32 v153, 0\n\t"
      "global_store_dwordx2 v153, v[138:139], s[34:35] sc1\n\t"
      "Lnoe7_%=:\n\t"
      "s_mov_b64 exec, -1\n\t"
      "Lnofl6_%=:\n\t"
      "s_waitcnt lgkmcnt(8)\n\t"
      "s_add_u32 m0, s70, 18432\n\t"
      "s_add_u32 s99, s67, 65536\n\t"
      "buffer_load_dwordx4 v132, s[40:43], s67 offen lds\n\t"
      "buffer_load_dwordx4 v133, s[40:43], s67 offen offset:1024 lds\n\t"
      "s_cmp_eq_u32 s99, s69\n\t"
      "s_cselect_b32 s67, s68, s99\n\t"
      "s_waitcnt lgkmcnt(4)\n\t"
      "s_add_u32 m0, s70, 20480\n\t"
      "s_add_u32 s99, s67, 65536\n\t"
      "buffer_load_dwordx4 v132, s[40:43], s67 offen lds\n\t"
      "buffer_load_dwordx4 v133, s[40:43], s67 offen offset:1024 lds\n\t"
      "s_cmp_eq_u32 s99, s69\n\t"
      "s_cselect_b32 s67, s68, s99\n\t"
      "ds_read_b64_tr_b16 a[96:97], v134 offset:24576\n\t"
      "ds_read_b64_tr_b16 a[98:99], v134 offset:25088\n\t"
      "ds_read_b64_tr_b16 a[100:101], v135 offset:24576\n\t"
      "ds_read_b64_tr_b16 a[102:103], v135 offset:25088\n\t"
      "s_waitcnt lgkmcnt(4)\n\t"
      "s_add_u32 m0, s70, 22528\n\t"
      "s_add_u32 s99, s67, 65536\n\t"
      "buffer_load_dwordx4 v132, s[40:43], s67 offen lds\n\t"
      "buffer_load_dwordx4 v133, s[40:43], s67 offen offset:1024 lds\n\t"
      "s_cmp_eq_u32 s99, s69\n\t"
      "s_cselect_b32 s67, s68, s99\n\t"
      "ds_read_b64_tr_b16 a[104:105], v134 offset:26624\n\t"
      "ds_read_b64_tr_b16 a[106:107], v134 offset:27136\n\t"
      "ds_read_b64_tr_b16 a[108:109], v135 offset:26624\n\t"
      "ds_read_b64_tr_b16 a[110:111], v135 offset:27136\n\t"
      "s_waitcnt lgkmcnt(4)\n\t"
      "s_add_u32 m0, s70, 24576\n\t"
      "s_add_u32 s99, s67, 65536\n\t"
      "buffer_load_dwordx4 v132, s[40:43], s67 offen lds\n\t"
      "buffer_load_dwordx4 v133, s[40:43], s67 offen offset:1024 lds\n\t"
      "s_cmp_eq_u32 s99, s69\n\t"
      "s_cselect_b32 s67, s68, s99\n\t"
      "ds_read_b64_tr_b16 a[112:113], v134 offset:0\n\t"
      "ds_read_b64_tr_b16 a[114:115], v134 offset:512\n\t"
      "ds_read_b64_tr_b16 a[116:117], v135 offset:0\n\t"
      "ds_read_b64_tr_b16 a[118:119], v135 offset:512\n\t"
      "s_waitcnt lgkmcnt(4)\n\t"
      "s_add_u32 m0, s70, 26624\n\t"
      "s_add_u32 s99, s67, 65536\n\t"
      "buffer_load_dwordx4 v132, s[40:43], s67 offen lds\n\t"
      "buffer_load_dwordx4 v133, s[40:43], s67 offen offset:1024 lds\n\t"
      "s_cmp_eq_u32 s99, s69\n\t"
      "s_cselect_b32 s67, s68, s99\n\t"
      "ds_read_b64_tr_b16 a[120:121], v134 offset:2048\n\t"
      "ds_read_b64_tr_b16 a[122:123], v134 offset:2560\n\t"
      "ds_read_b64_tr_b16 a[124:125], v135 offset:2048\n\t"
      "ds_read_b64_tr_b16 a[126:127], v135 offset:2560\n\t"
      "ds_read_b64_tr_b16 a[128:129], v134 offset:4096\n\t"
      "ds_read_b64_tr_b16 a[130:131], v134 offset:4608\n\t"
      "ds_read_b64_tr_b16 a[132:133], v135 offset:4096\n\t"
      "ds_read_b64_tr_b16 a[134:135], v135 offset:4608\n\t"
      "s_waitcnt lgkmcnt(4)\n\t"
      "ds_read_b64_tr_b16 a[136:137], v134 offset:6144\n\t"
      "ds_read_b64_tr_b16 a[138:139], v134 offset:6656\n\t"
      "ds_read_b64_tr_b16 a[140:141], v135 offset:6144\n\t"
      "ds_read_b64_tr_b16 a[142:143], v135 offset:6656\n\t"
      "ds_read_b64_tr_b16 a[144:145], v134 offset:8192\n\t"
      "ds_read_b64_tr_b16 a[146:147], v134 offset:8704\n\t"
      "ds_read_b64_tr_b16 a[148:149], v135 offset:8192\n\t"
      "ds_read_b64_tr_b16 a[150:151], v135 offset:8704\n\t"
      "s_waitcnt lgkmcnt(4)\n\t"
      "ds_read_b64_tr_b16 a[152:153], v134 offset:10240\n\t"
      "ds_read_b64_tr_b16 a[154:155], v134 offset:10752\n\t"
      "ds_read_b64_tr_b16 a[156:157], v135 offset:10240\n\t"
      "ds_read_b64_tr_b16 a[158:159], v135 offset:10752\n\t"
      "s_waitcnt vmcnt(14)\n\t"
      "ds_read_b64_tr_b16 a[160:161], v134 offset:12288\n\t"
      "ds_read_b64_tr_b16 a[162:163], v134 offset:12800\n\t"
      "ds_read_b64_tr_b16 a[164:165], v135 offset:12288\n\t"
      "ds_read_b64_tr_b16 a[166:167], v135 offset:12800\n\t"
      "s_waitcnt vmcnt(12)\n\t"
      "s_waitcnt lgkmcnt(4)\n\t"
      "ds_read_b64_tr_b16 a[168:169], v134 offset:14336\n\t"
      "ds_read_b64_tr_b16 a[170:171], v134 offset:14848\n\t"
      "ds_read_b64_tr_b16 a[172:173], v135 offset:14336\n\t"
      "ds_read_b64_tr_b16 a[174:175], v135 offset:14848\n\t"
      "s_waitcnt vmcnt(10)\n\t"
      "ds_read_b64_tr_b16 a[176:177], v134 offset:16384\n\t"
      "ds_read_b64_tr_b16 a[178:179], v134 offset:16896\n\t"
      "ds_read_b64_tr_b16 a[180:181], v135 offset:16384\n\t"
      "ds_read_b64_tr_b16 a[182:183], v135 offset:16896\n\t"
      "s_waitcnt vmcnt(8)\n\t"
      "s_waitcnt lgkmcnt(4)\n\t"
      "ds_read_b64_tr_b16 a[184:185], v134 offset:18432\n\t"
      "ds_read_b64_tr_b16 a[186:187], v134 offset:18944\n\t"
      "ds_read_b64_tr_b16 a[188:189], v135 offset:18432\n\t"
      "ds_read_b64_tr_b16 a[190:191], v135 offset:18944\n\t"
      "s_waitcnt vmcnt(6)\n\t"
      "ds_read_b64_tr_b16 a[192:193], v134 offset:20480\n\t"
      "ds_read_b64_tr_b16 a[194:195], v134 offset:20992\n\t"
      "ds_read_b64_tr_b16 a[196:197], v135 offset:20480\n\t"
      "ds_read_b64_tr_b16 a[198:199], v135 offset:20992\n\t"
      "s_waitcnt vmcnt(4)\n\t"
      "s_waitcnt lgkmcnt(4)\n\t"
      "ds_read_b64_tr_b16 a[200:201], v134 offset:22528\n\t"
      "ds_read_b64_tr_b16 a[202:203], v134 offset:23040\n\t"
      "ds_read_b64_tr_b16 a[204:205], v135 offset:22528\n\t"
      "ds_read_b64_tr_b16 a[206:207], v135 offset:23040\n\t"
      "s_waitcnt vmcnt(2)\n\t"
      "ds_read_b64_tr_b16 a[208:209], v134 offset:24576\n\t"
      "ds_read_b64_tr_b16 a[210:211], v134 offset:25088\n\t"
      "ds_read_b64_tr_b16 a[212:213], v135 offset:24576\n\t"
      "ds_read_b64_tr_b16 a[214:215], v135 offset:25088\n\t"
      "s_waitcnt vmcnt(0)\n\t"
      "s_waitcnt lgkmcnt(4)\n\t"
      "ds_read_b64_tr_b16 a[216:217], v134 offset:26624\n\t"
      "ds_read_b64_tr_b16 a[218:219], v134 offset:27136\n\t"
      "ds_read_b64_tr_b16 a[220:221], v135 offset:26624\n\t"
      "ds_read_b64_tr_b16 a[222:223], v135 offset:27136\n\t"
      "s_waitcnt lgkmcnt(0)\n\t"
      "s_branch Lw2done%=\n\t"
      "Lidle%=:\n\t"
      "s_cmp_lg_u32 s56, 3\n\t"
      "s_cbranch_scc1 Lnopf8_%=\n\t"
      "s_cmp_ge_u32 s58, (kend%=-kstart%=)>>10\n\t"
      "s_cbranch_scc1 Lnopf8_%=\n\t"
      "s_lshl_b32 s72, s58, 10\n\t"
      "s_add_u32 s74, s16, s72\n\t"
      "s_addc_u32 s75, s17, 0\n\t"
      "global_load_dwordx4 v[156:159], v154, s[74:75]\n\t"
      "Lnopf8_%=:\n\t"
      "v_lshrrev_b32 v168, 3, v128\n\t"
      "v_bfe_u32 v169, v128, 4, 1\n\t"
      "v_lshlrev_b32 v169, 2, v169\n\t"
      "v_and_b32 v170, 7, v128\n\t"
      "v_xor_b32 v170, v170, v169\n\t"
      "v_lshlrev_b32 v170, 4, v170\n\t"
      "v_bfe_u32 v169, v128, 5, 1\n\t"
      "v_lshlrev_b32 v169, 3, v169\n\t"
      "v_and_b32 v171, 3, v168\n\t"
      "v_add_u32 v171, v171, v169\n\t"
      "v_lshlrev_b32 v171, 12, v171\n\t"
      "v_add_u32 v132, v171, v170\n\t"
      "v_add_u32 v133, 15360, v132\n\t"
      "s_mov_b32 s40, s26\n\t"
      "s_and_b32 s41, s27, 0xffff\n\t"
      "s_mov_b32 s42, 7340032\n\t"
      "s_mov_b32 s43, 0x20000\n\t"
      "s_mul_i32 s68, s56, 1835008\n\t"
      "s_lshl_b32 s72, s62, 7\n\t"
      "s_add_u32 s68, s68, s72\n\t"
      "s_add_u32 s69, s68, 1835008\n\t"
      "s_mul_i32 s72, s63, 7\n\t"
      "s_lshr_b32 s72, s72, 1\n\t"
      "s_mul_i32 s72, s72, 65536\n\t"
      "s_add_u32 s67, s68, s72\n\t"
      "s_sub_u32 s72, s58, 28\n\t"
      "s_lshl_b32 s72, s72, 2\n\t"
      "s_add_u32 s72, s72, s56\n\t"
      "s_mul_i32 s72, s72, 458752\n\t"
      "s_lshl_b32 s73, s59, 9\n\t"
      "s_add_u32 s64, s72, s73\n\t"
      "v_lshrrev_b32 v168, 5, v128\n\t"
      "v_lshlrev_b32 v168, 12, v168\n\t"
      "v_and_b32 v169, 31, v128\n\t"
      "v_lshl_add_u32 v130, v169, 4, v168\n\t"
      "buffer_load_dwordx4 v[96:99], v130, s[40:43], s64 offen\n\t"
      "s_add_u32 s64, s64, 8192\n\t"
      "buffer_load_dwordx4 v[100:103], v130, s[40:43], s64 offen\n\t"
      "s_add_u32 s64, s64, 8192\n\t"
      "buffer_load_dwordx4 v[104:107], v130, s[40:43], s64 offen\n\t"
      "s_add_u32 s64, s64, 8192\n\t"
      "buffer_load_dwordx4 v[108:111], v130, s[40:43], s64 offen\n\t"
      "s_add_u32 s64, s64, 8192\n\t"
      "buffer_load_dwordx4 v[112:115], v130, s[40:43], s64 offen\n\t"
      "s_add_u32 s64, s64, 8192\n\t"
      "buffer_load_dwordx4 v[116:119], v130, s[40:43], s64 offen\n\t"
      "s_add_u32 s64, s64, 8192\n\t"
      "buffer_load_dwordx4 v[120:123], v130, s[40:43], s64 offen\n\t"
      "s_add_u32 s64, s64, 8192\n\t"
      "buffer_load_dwordx4 v[124:127], v130, s[40:43], s64 offen\n\t"
      "s_add_u32 s64, s64, 8192\n\t"
      "s_lshl_b32 s72, s62, 6\n\t"
      "s_lshl_b32 s73, s79, 5\n\t"
      "s_add_u32 s72, s72, s73\n\t"
      "v_and_b32 v168, 31, v128\n\t"
      "v_add_u32 v168, s72, v168\n\t"
      "v_lshlrev_b32 v168, 1, v168\n\t"
      "global_load_ushort v148, v168, s[28:29]\n\t"
      "v_lshrrev_b32 v168, 3, v128\n\t"
      "v_lshlrev_b32 v168, 10, v168\n\t"
      "v_bfe_u32 v169, v128, 2, 1\n\t"
      "v_lshl_add_u32 v168, v169, 9, v168\n\t"
      "v_lshlrev_b32 v170, 1, v169\n\t"
      "v_and_b32 v169, 1, v128\n\t"
      "v_lshl_add_u32 v168, v169, 8, v168\n\t"
      "v_add_u32 v170, v170, v169\n\t"
      "v_bfe_u32 v169, v128, 1, 1\n\t"
      "v_lshl_add_u32 v168, v169, 6, v168\n\t"
      "s_lshl_b32 s72, s56, 2\n\t"
      "s_add_u32 s72, s72, %[ldsb]\n\t"
      "s_add_u32 s72, s72, 114688\n\t"
      "v_add_u32 v168, s72, v168\n\t"
      "v_xor_b32 v169, 0, v170\n\t"
      "v_lshl_add_u32 v192, v169, 4, v168\n\t"
      "v_xor_b32 v169, 1, v170\n\t"
      "v_lshl_add_u32 v193, v169, 4, v168\n\t"
      "v_xor_b32 v169, 2, v170\n\t"
      "v_lshl_add_u32 v194, v169, 4, v168\n\t"
      "v_xor_b32 v169, 3, v170\n\t"
      "v_lshl_add_u32 v195, v169, 4, v168\n\t"
      "v_bfe_u32 v169, v129, 4, 2\n\t"
      "v_xor_b32 v169, v129, v169\n\t"
      "v_lshlrev_b32 v169, 4, v169\n\t"
      "s_add_u32 s72, %[ldsb], 114688\n\t"
      "v_add_u32 v196, s72, v169\n\t"
      "v_lshrrev_b32 v169, 5, v128\n\t"
      "v_lshlrev_b32 v169, 10, v169\n\t"
      "v_and_b32 v170, 31, v128\n\t"
      "v_lshl_add_u32 v169, v170, 4, v169\n\t"
      "s_lshl_b32 s72, s56, 18\n\t"
      "v_add_u32 v197, s72, v169\n\t"
      "s_lshr_b32 s76, s57, 1\n\t"
      "s_lshl_b32 s76, s76, 11\n\t"
      "s_and_b32 s77, s57, 1\n\t"
      "s_lshl_b32 s77, s77, 9\n\t"
      "s_add_u32 s76, s76, s77\n\t"
      "s_add_u32 s77, s76, 1048576\n\t"
      "s_lshl_b32 s72, s57, 5\n\t"
      "s_add_u32 s72, s72, 4096\n\t"
      "v_mov_b32 v153, s72\n\t"
      "v_lshrrev_b32 v168, 5, v128\n\t"
      "v_lshlrev_b32 v168, 10, v168\n\t"
      "v_bfe_u32 v169, v128, 2, 2\n\t"
      "v_lshl_add_u32 v168, v169, 7, v168\n\t"
      "v_bfe_u32 v169, v128, 4, 1\n\t"
      "v_lshl_add_u32 v168, v169, 5, v168\n\t"
      "v_and_b32 v169, 3, v128\n\t"
      "v_lshl_add_u32 v168, v169, 3, v168\n\t"
      "v_add_u32 v168, s70, v168\n\t"
      "v_bfe_u32 v169, v128, 3, 1\n\t"
      "v_xor_b32 v169, s79, v169\n\t"
      "v_lshl_add_u32 v134, v169, 6, v168\n\t"
      "v_xor_b32 v169, 1, v169\n\t"
      "v_lshl_add_u32 v135, v169, 6, v168\n\t"
      "s_add_u32 s44, s34, 131072\n\t"
      "s_addc_u32 s45, s35, 0\n\t"
      "s_and_b32 s45, s45, 0xffff\n\t"
      "s_mov_b32 s46, 2097152\n\t"
      "s_mov_b32 s47, 0x20000\n\t"
      "s_lshl_b32 s72, s78, 10\n\t"
      "v_add_u32 v136, s72, v154\n\t"
      "s_xor_b32 s72, s72, 1024\n\t"
      "v_add_u32 v137, s72, v154\n\t"
      "s_lshl_b32 s72, s56, 11\n\t"
      "s_add_u32 s72, s72, 4096\n\t"
      "v_lshlrev_b32 v198, 5, v128\n\t"
      "v_add_u32 v198, s72, v198\n\t"
      "s_add_u32 s48, s34, 2228224\n\t"
      "s_addc_u32 s49, s35, 0\n\t"
      "s_and_b32 s49, s49, 0xffff\n\t"
      "s_mov_b32 s50, 1835008\n\t"
      "s_mov_b32 s51, 0x20000\n\t"
      "s_mov_b32 s52, s30\n\t"
      "s_and_b32 s53, s31, 0xffff\n\t"
      "s_mov_b32 s54, 2097152\n\t"
      "s_mov_b32 s55, 0x20000\n\t"
      "s_xor_b32 s72, s56, 1\n\t"
      "s_mul_i32 s72, s72, 12288\n\t"
      "s_add_u32 s72, s72, 114688\n\t"
      "s_add_u32 s72, s72, %[ldsb]\n\t"
      "v_add_u32 v149, s72, v154\n\t"
      "s_xor_b32 s72, s56, 2\n\t"
      "s_mul_i32 s72, s72, 12288\n\t"
      "s_add_u32 s72, s72, 118784\n\t"
      "s_add_u32 s72, s72, %[ldsb]\n\t"
      "v_add_u32 v150, s72, v154\n\t"
      "s_xor_b32 s72, s56, 3\n\t"
      "s_mul_i32 s72, s72, 12288\n\t"
      "s_add_u32 s72, s72, 122880\n\t"
      "s_add_u32 s72, s72, %[ldsb]\n\t"
      "v_add_u32 v151, s72, v154\n\t"
      "s_mul_i32 s72, s56, 12288\n\t"
      "s_add_u32 s72, s72, 114688\n\t"
      "s_add_u32 s72, s72, %[ldsb]\n\t"
      "v_add_u32 v152, s72, v154\n\t"
      "s_lshl_b32 s72, s62, 6\n\t"
      "s_lshl_b32 s73, s79, 5\n\t"
      "s_add_u32 s72, s72, s73\n\t"
      "s_lshl_b32 s72, s72, 10\n\t"
      "s_lshl_b32 s73, s63, 7\n\t"
      "s_add_u32 s72, s72, s73\n\t"
      "s_lshl_b32 s73, s78, 6\n\t"
      "s_add_u32 s95, s72, s73\n\t"
      "s_lshl_b32 s72, s62, 6\n\t"
      "s_lshl_b32 s73, s56, 4\n\t"
      "s_add_u32 s72, s72, s73\n\t"
      "s_lshl_b32 s72, s72, 10\n\t"
      "s_lshl_b32 s73, s63, 7\n\t"
      "s_add_u32 s95, s72, s73\n\t"
      "v_lshrrev_b32 v168, 3, v128\n\t"
      "v_and_b32 v169, 7, v128\n\t"
      "v_lshlrev_b32 v169, 4, v169\n\t"
      "v_lshl_add_u32 v155, v168, 10, v169\n\t"
      "s_mul_i32 s72, s56, 2304\n\t"
      "s_add_u32 s72, s72, %[ldsb]\n\t"
      "v_mul_u32_u24 v168, 144, v168\n\t"
      "v_add3_u32 v203, v168, v169, s72\n\t"
      "s_mul_i32 s72, s79, 4608\n\t"
      "s_lshl_b32 s73, s78, 6\n\t"
      "s_add_u32 s72, s72, s73\n\t"
      "s_add_u32 s72, s72, %[ldsb]\n\t"
      "v_and_b32 v168, 31, v128\n\t"
      "v_mul_u32_u24 v168, 144, v168\n\t"
      "v_lshrrev_b32 v169, 5, v128\n\t"
      "v_lshlrev_b32 v169, 5, v169\n\t"
      "v_add3_u32 v202, v168, v169, s72\n\t"
      "s_waitcnt vmcnt(10)\n\t"
      "v_lshlrev_b32 v64, 16, v160\n\t"
      "v_lshlrev_b32 v65, 16, v164\n\t"
      "v_and_b32 v66, 0xffff0000, v160\n\t"
      "v_and_b32 v67, 0xffff0000, v164\n\t"
      "v_lshlrev_b32 v68, 16, v161\n\t"
      "v_lshlrev_b32 v69, 16, v165\n\t"
      "v_and_b32 v70, 0xffff0000, v161\n\t"
      "v_and_b32 v71, 0xffff0000, v165\n\t"
      "v_lshlrev_b32 v72, 16, v162\n\t"
      "v_lshlrev_b32 v73, 16, v166\n\t"
      "v_and_b32 v74, 0xffff0000, v162\n\t"
      "v_and_b32 v75, 0xffff0000, v166\n\t"
      "v_lshlrev_b32 v76, 16, v163\n\t"
      "v_lshlrev_b32 v77, 16, v167\n\t"
      "v_and_b32 v78, 0xffff0000, v163\n\t"
      "v_and_b32 v79, 0xffff0000, v167\n\t"
      "v_pk_add_f32 v[80:81], v[64:65], v[66:67]\n\t"
      "v_pk_add_f32 v[82:83], v[68:69], v[70:71]\n\t"
      "v_pk_add_f32 v[84:85], v[72:73], v[74:75]\n\t"
      "v_pk_add_f32 v[86:87], v[76:77], v[78:79]\n\t"
      "v_pk_add_f32 v[80:81], v[80:81], v[82:83]\n\t"
      "v_pk_add_f32 v[84:85], v[84:85], v[86:87]\n\t"
      "v_pk_add_f32 v[80:81], v[80:81], v[84:85]\n\t"
      "s_nop 0\n\t"
      "s_nop 0\n\t"
      "v_add_f32_dpp v80, v80, v80 row_shr:1 row_mask:0xf bank_mask:0xf bound_ctrl:1\n\t"
      "v_add_f32_dpp v81, v81, v81 row_shr:1 row_mask:0xf bank_mask:0xf bound_ctrl:1\n\t"
      "s_nop 0\n\t"
      "v_add_f32_dpp v80, v80, v80 row_shr:2 row_mask:0xf bank_mask:0xf bound_ctrl:1\n\t"
      "v_add_f32_dpp v81, v81, v81 row_shr:2 row_mask:0xf bank_mask:0xf bound_ctrl:1\n\t"
      "s_nop 0\n\t"
      "v_add_f32_dpp v80, v80, v80 row_shr:4 row_mask:0xf bank_mask:0xf bound_ctrl:1\n\t"
      "v_add_f32_dpp v81, v81, v81 row_shr:4 row_mask:0xf bank_mask:0xf bound_ctrl:1\n\t"
      "s_nop 0\n\t"
      "v_add_f32_dpp v80, v80, v80 row_shr:8 row_mask:0xf bank_mask:0xf bound_ctrl:1\n\t"
      "v_add_f32_dpp v81, v81, v81 row_shr:8 row_mask:0xf bank_mask:0xf bound_ctrl:1\n\t"
      "s_nop 0\n\t"
      "v_add_f32_dpp v80, v80, v80 row_bcast:15 row_mask:0xa bank_mask:0xf\n\t"
      "v_add_f32_dpp v81, v81, v81 row_bcast:15 row_mask:0xa bank_mask:0xf\n\t"
      "s_nop 0\n\t"
      "v_add_f32_dpp v80, v80, v80 row_bcast:31 row_mask:0xc bank_mask:0xf\n\t"
      "v_add_f32_dpp v81, v81, v81 row_bcast:31 row_mask:0xc bank_mask:0xf\n\t"
      "s_nop 0\n\t"
      "v_readlane_b32 s72, v80, 63\n\t"
      "v_readlane_b32 s73, v81, 63\n\t"
      "s_nop 1\n\t"
      "v_mov_b32 v90, s72\n\t"
      "v_mov_b32 v91, s73\n\t"
      "v_mul_f32 v90, 0xbb000000, v90\n\t"
      "v_mul_f32 v91, 0xbb000000, v91\n\t"
      "v_pk_add_f32 v[64:65], v[64:65], v[90:91]\n\t"
      "v_pk_add_f32 v[66:67], v[66:67], v[90:91]\n\t"
      "v_pk_add_f32 v[68:69], v[68:69], v[90:91]\n\t"
      "v_pk_add_f32 v[70:71], v[70:71], v[90:91]\n\t"
      "v_pk_add_f32 v[72:73], v[72:73], v[90:91]\n\t"
      "v_pk_add_f32 v[74:75], v[74:75], v[90:91]\n\t"
      "v_pk_add_f32 v[76:77], v[76:77], v[90:91]\n\t"
      "v_pk_add_f32 v[78:79], v[78:79], v[90:91]\n\t"
      "v_pk_mul_f32 v[80:81], v[64:65], v[64:65]\n\t"
      "v_pk_mul_f32 v[82:83], v[66:67], v[66:67]\n\t"
      "v_pk_fma_f32 v[80:81], v[68:69], v[68:69], v[80:81]\n\t"
      "v_pk_fma_f32 v[82:83], v[70:71], v[70:71], v[82:83]\n\t"
      "v_pk_fma_f32 v[80:81], v[72:73], v[72:73], v[80:81]\n\t"
      "v_pk_fma_f32 v[82:83], v[74:75], v[74:75], v[82:83]\n\t"
      "v_pk_fma_f32 v[80:81], v[76:77], v[76:77], v[80:81]\n\t"
      "v_pk_fma_f32 v[82:83], v[78:79], v[78:79], v[82:83]\n\t"
      "v_pk_add_f32 v[80:81], v[80:81], v[82:83]\n\t"
      "s_nop 0\n\t"
      "s_nop 0\n\t"
      "v_add_f32_dpp v80, v80, v80 row_shr:1 row_mask:0xf bank_mask:0xf bound_ctrl:1\n\t"
      "v_add_f32_dpp v81, v81, v81 row_shr:1 row_mask:0xf bank_mask:0xf bound_ctrl:1\n\t"
      "s_nop 0\n\t"
      "v_add_f32_dpp v80, v80, v80 row_shr:2 row_mask:0xf bank_mask:0xf bound_ctrl:1\n\t"
      "v_add_f32_dpp v81, v81, v81 row_shr:2 row_mask:0xf bank_mask:0xf bound_ctrl:1\n\t"
      "s_nop 0\n\t"
      "v_add_f32_dpp v80, v80, v80 row_shr:4 row_mask:0xf bank_mask:0xf bound_ctrl:1\n\t"
      "v_add_f32_dpp v81, v81, v81 row_shr:4 row_mask:0xf bank_mask:0xf bound_ctrl:1\n\t"
      "s_nop 0\n\t"
      "v_add_f32_dpp v80, v80, v80 row_shr:8 row_mask:0xf bank_mask:0xf bound_ctrl:1\n\t"
      "v_add_f32_dpp v81, v81, v81 row_shr:8 row_mask:0xf bank_mask:0xf bound_ctrl:1\n\t"
      "s_nop 0\n\t"
      "v_add_f32_dpp v80, v80, v80 row_bcast:15 row_mask:0xa bank_mask:0xf\n\t"
      "v_add_f32_dpp v81, v81, v81 row_bcast:15 row_mask:0xa bank_mask:0xf\n\t"
      "s_nop 0\n\t"
      "v_add_f32_dpp v80, v80, v80 row_bcast:31 row_mask:0xc bank_mask:0xf\n\t"
      "v_add_f32_dpp v81, v81, v81 row_bcast:31 row_mask:0xc bank_mask:0xf\n\t"
      "s_nop 0\n\t"
      "v_readlane_b32 s72, v80, 63\n\t"
      "v_readlane_b32 s73, v81, 63\n\t"
      "s_nop 1\n\t"
      "v_mov_b32 v90, s72\n\t"
      "v_mov_b32 v91, s73\n\t"
      "v_mov_b32 v92, 0x3727c5ac\n\t"
      "v_fmac_f32 v92, 0x3b000000, v90\n\t"
      "v_mov_b32 v93, 0x3727c5ac\n\t"
      "v_fmac_f32 v93, 0x3b000000, v91\n\t"
      "v_rsq_f32 v90, v92\n\t"
      "v_rsq_f32 v91, v93\n\t"
      "s_nop 0\n\t"
      "v_pk_mul_f32 v[64:65], v[64:65], v[90:91]\n\t"
      "v_pk_mul_f32 v[66:67], v[66:67], v[90:91]\n\t"
      "v_pk_mul_f32 v[68:69], v[68:69], v[90:91]\n\t"
      "v_pk_mul_f32 v[70:71], v[70:71], v[90:91]\n\t"
      "v_pk_mul_f32 v[72:73], v[72:73], v[90:91]\n\t"
      "v_pk_mul_f32 v[74:75], v[74:75], v[90:91]\n\t"
      "v_pk_mul_f32 v[76:77], v[76:77], v[90:91]\n\t"
      "v_pk_mul_f32 v[78:79], v[78:79], v[90:91]\n\t"
      "v_cvt_pk_bf16_f32 v80, v64, v65\n\t"
      "v_cvt_pk_bf16_f32 v81, v66, v67\n\t"
      "v_cvt_pk_bf16_f32 v82, v68, v69\n\t"
      "v_cvt_pk_bf16_f32 v83, v70, v71\n\t"
      "v_cvt_pk_bf16_f32 v84, v72, v73\n\t"
      "v_cvt_pk_bf16_f32 v85, v74, v75\n\t"
      "v_cvt_pk_bf16_f32 v86, v76, v77\n\t"
      "v_cvt_pk_bf16_f32 v87, v78, v79\n\t"
      "ds_write_b32 v192, v80 offset:0\n\t"
      "ds_write_b32 v193, v81 offset:0\n\t"
      "ds_write_b32 v194, v82 offset:0\n\t"
      "ds_write_b32 v195, v83 offset:0\n\t"
      "ds_write_b32 v192, v84 offset:128\n\t"
      "ds_write_b32 v193, v85 offset:128\n\t"
      "ds_write_b32 v194, v86 offset:128\n\t"
      "ds_write_b32 v195, v87 offset:128\n\t"
      "s_waitcnt lgkmcnt(0)\n\t"
      "s_barrier\n\t"
      "ds_read_b128 v[64:67], v196\n\t"
      "ds_read_b128 v[68:71], v196 offset:4096\n\t"
      "s_waitcnt lgkmcnt(1)\n\t"
      "buffer_store_dwordx4 v[64:67], v197, s[44:47], s76 offen sc1\n\t"
      "s_waitcnt lgkmcnt(0)\n\t"
      "buffer_store_dwordx4 v[68:71], v197, s[44:47], s77 offen sc1\n\t"
      "s_waitcnt vmcnt(0)\n\t"
      "v_add_co_u32 v138, vcc, 1, v138\n\t"
      "s_nop 1\n\t"
      "v_addc_co_u32 v139, vcc, 0, v139, vcc\n\t"
      "s_barrier\n\t"
      "s_cmp_lg_u32 s56, 0\n\t"
      "s_cbranch_scc1 Lnofl9_%=\n\t"
      "s_mov_b64 exec, 1\n\t"
      "global_store_dwordx2 v153, v[138:139], s[34:35] sc1\n\t"
      "s_mov_b64 exec, -1\n\t"
      "Lnofl9_%=:\n\t"
      "buffer_load_dwordx4 v[96:99], v130, s[40:43], s64 offen\n\t"
      "s_add_u32 s64, s64, 8192\n\t"
      "buffer_load_dwordx4 v[100:103], v130, s[40:43], s64 offen\n\t"
      "s_add_u32 s64, s64, 8192\n\t"
      "buffer_load_dwordx4 v[104:107], v130, s[40:43], s64 offen\n\t"
      "s_add_u32 s64, s64, 8192\n\t"
      "buffer_load_dwordx4 v[108:111], v130, s[40:43], s64 offen\n\t"
      "s_add_u32 s64, s64, 8192\n\t"
      "buffer_load_dwordx4 v[112:115], v130, s[40:43], s64 offen\n\t"
      "s_add_u32 s64, s64, 8192\n\t"
      "buffer_load_dwordx4 v[116:119], v130, s[40:43], s64 offen\n\t"
      "s_add_u32 s64, s64, 8192\n\t"
      "buffer_load_dwordx4 v[120:123], v130, s[40:43], s64 offen\n\t"
      "s_add_u32 s64, s64, 8192\n\t"
      "buffer_load_dwordx4 v[124:127], v130, s[40:43], s64 offen\n\t"
      "s_add_u32 s64, s64, 8192\n\t"
      "s_mov_b32 s94, 5\n\t"
      "Lpf10_%=:\n\t"
      "s_waitcnt vmcnt(7)\n\t"
      "buffer_load_dwordx4 v[96:99], v130, s[40:43], s64 offen\n\t"
      "s_add_u32 s64, s64, 8192\n\t"
      "s_waitcnt vmcnt(7)\n\t"
      "buffer_load_dwordx4 v[100:103], v130, s[40:43], s64 offen\n\t"
      "s_add_u32 s64, s64, 8192\n\t"
      "s_waitcnt vmcnt(7)\n\t"
      "buffer_load_dwordx4 v[104:107], v130, s[40:43], s64 offen\n\t"
      "s_add_u32 s64, s64, 8192\n\t"
      "s_waitcnt vmcnt(7)\n\t"
      "buffer_load_dwordx4 v[108:111], v130, s[40:43], s64 offen\n\t"
      "s_add_u32 s64, s64, 8192\n\t"
      "s_waitcnt vmcnt(7)\n\t"
      "buffer_load_dwordx4 v[112:115], v130, s[40:43], s64 offen\n\t"
      "s_add_u32 s64, s64, 8192\n\t"
      "s_waitcnt vmcnt(7)\n\t"
      "buffer_load_dwordx4 v[116:119], v130, s[40:43], s64 offen\n\t"
      "s_add_u32 s64, s64, 8192\n\t"
      "s_waitcnt vmcnt(7)\n\t"
      "buffer_load_dwordx4 v[120:123], v130, s[40:43], s64 offen\n\t"
      "s_add_u32 s64, s64, 8192\n\t"
      "s_waitcnt vmcnt(7)\n\t"
      "buffer_load_dwordx4 v[124:127], v130, s[40:43], s64 offen\n\t"
      "s_add_u32 s64, s64, 8192\n\t"
      "s_sub_u32 s94, s94, 1\n\t"
      "s_cmp_lg_u32 s94, 0\n\t"
      "s_cbranch_scc1 Lpf10_%=\n\t"
      "s_waitcnt vmcnt(0)\n\t"
      "s_add_u32 m0, s70, 0\n\t"
      "s_add_u32 s99, s67, 65536\n\t"
      "buffer_load_dwordx4 v132, s[40:43], s67 offen lds\n\t"
      "buffer_load_dwordx4 v133, s[40:43], s67 offen offset:1024 lds\n\t"
      "s_cmp_eq_u32 s99, s69\n\t"
      "s_cselect_b32 s67, s68, s99\n\t"
      "s_add_u32 m0, s70, 2048\n\t"
      "s_add_u32 s99, s67, 65536\n\t"
      "buffer_load_dwordx4 v132, s[40:43], s67 offen lds\n\t"
      "buffer_load_dwordx4 v133, s[40:43], s67 offen offset:1024 lds\n\t"
      "s_cmp_eq_u32 s99, s69\n\t"
      "s_cselect_b32 s67, s68, s99\n\t"
      "s_add_u32 m0, s70, 4096\n\t"
      "s_add_u32 s99, s67, 65536\n\t"
      "buffer_load_dwordx4 v132, s[40:43], s67 offen lds\n\t"
      "buffer_load_dwordx4 v133, s[40:43], s67 offen offset:1024 lds\n\t"
      "s_cmp_eq_u32 s99, s69\n\t"
      "s_cselect_b32 s67, s68, s99\n\t"
      "s_add_u32 m0, s70, 6144\n\t"
      "s_add_u32 s99, s67, 65536\n\t"
      "buffer_load_dwordx4 v132, s[40:43], s67 offen lds\n\t"
      "buffer_load_dwordx4 v133, s[40:43], s67 offen offset:1024 lds\n\t"
      "s_cmp_eq_u32 s99, s69\n\t"
      "s_cselect_b32 s67, s68, s99\n\t"
      "s_add_u32 m0, s70, 8192\n\t"
      "s_add_u32 s99, s67, 65536\n\t"
      "buffer_load_dwordx4 v132, s[40:43], s67 offen lds\n\t"
      "buffer_load_dwordx4 v133, s[40:43], s67 offen offset:1024 lds\n\t"
      "s_cmp_eq_u32 s99, s69\n\t"
      "s_cselect_b32 s67, s68, s99\n\t"
      "s_add_u32 m0, s70, 10240\n\t"
      "s_add_u32 s99, s67, 65536\n\t"
      "buffer_load_dwordx4 v132, s[40:43], s67 offen lds\n\t"
      "buffer_load_dwordx4 v133, s[40:43], s67 offen offset:1024 lds\n\t"
      "s_cmp_eq_u32 s99, s69\n\t"
      "s_cselect_b32 s67, s68, s99\n\t"
      "s_add_u32 m0, s70, 12288\n\t"
      "s_add_u32 s99, s67, 65536\n\t"
      "buffer_load_dwordx4 v132, s[40:43], s67 offen lds\n\t"
      "buffer_load_dwordx4 v133, s[40:43], s67 offen offset:1024 lds\n\t"
      "s_cmp_eq_u32 s99, s69\n\t"
      "s_cselect_b32 s67, s68, s99\n\t"
      "s_add_u32 m0, s70, 14336\n\t"
      "s_add_u32 s99, s67, 65536\n\t"
      "buffer_load_dwordx4 v132, s[40:43], s67 offen lds\n\t"
      "buffer_load_dwordx4 v133, s[40:43], s67 offen offset:1024 lds\n\t"
      "s_cmp_eq_u32 s99, s69\n\t"
      "s_cselect_b32 s67, s68, s99\n\t"
      "s_waitcnt vmcnt(14)\n\t"
      "ds_read_b64_tr_b16 a[0:1], v134 offset:0\n\t"
      "ds_read_b64_tr_b16 a[2:3], v134 offset:512\n\t"
      "ds_read_b64_tr_b16 a[4:5], v135 offset:0\n\t"
      "ds_read_b64_tr_b16 a[6:7], v135 offset:512\n\t"
      "s_add_u32 m0, s70, 16384\n\t"
      "s_add_u32 s99, s67, 65536\n\t"
      "buffer_load_dwordx4 v132, s[40:43], s67 offen lds\n\t"
      "buffer_load_dwordx4 v133, s[40:43], s67 offen offset:1024 lds\n\t"
      "s_cmp_eq_u32 s99, s69\n\t"
      "s_cselect_b32 s67, s68, s99\n\t"
      "s_waitcnt vmcnt(14)\n\t"
      "ds_read_b64_tr_b16 a[8:9], v134 offset:2048\n\t"
      "ds_read_b64_tr_b16 a[10:11], v134 offset:2560\n\t"
      "ds_read_b64_tr_b16 a[12:13], v135 offset:2048\n\t"
      "ds_read_b64_tr_b16 a[14:15], v135 offset:2560\n\t"
      "s_add_u32 m0, s70, 18432\n\t"
      "s_add_u32 s99, s67, 65536\n\t"
      "buffer_load_dwordx4 v132, s[40:43], s67 offen lds\n\t"
      "buffer_load_dwordx4 v133, s[40:43], s67 offen offset:1024 lds\n\t"
      "s_cmp_eq_u32 s99, s69\n\t"
      "s_cselect_b32 s67, s68, s99\n\t"
      "s_waitcnt vmcnt(14)\n\t"
      "ds_read_b64_tr_b16 a[16:17], v134 offset:4096\n\t"
      "ds_read_b64_tr_b16 a[18:19], v134 offset:4608\n\t"
      "ds_read_b64_tr_b16 a[20:21], v135 offset:4096\n\t"
      "ds_read_b64_tr_b16 a[22:23], v135 offset:4608\n\t"
      "s_add_u32 m0, s70, 20480\n\t"
      "s_add_u32 s99, s67, 65536\n\t"
      "buffer_load_dwordx4 v132, s[40:43], s67 offen lds\n\t"
      "buffer_load_dwordx4 v133, s[40:43], s67 offen offset:1024 lds\n\t"
      "s_cmp_eq_u32 s99, s69\n\t"
      "s_cselect_b32 s67, s68, s99\n\t"
      "s_waitcnt vmcnt(14)\n\t"
      "s_waitcnt lgkmcnt(4)\n\t"
      "ds_read_b64_tr_b16 a[24:25], v134 offset:6144\n\t"
      "ds_read_b64_tr_b16 a[26:27], v134 offset:6656\n\t"
      "ds_read_b64_tr_b16 a[28:29], v135 offset:6144\n\t"
      "ds_read_b64_tr_b16 a[30:31], v135 offset:6656\n\t"
      "s_add_u32 m0, s70, 22528\n\t"
      "s_add_u32 s99, s67, 65536\n\t"
      "buffer_load_dwordx4 v132, s[40:43], s67 offen lds\n\t"
      "buffer_load_dwordx4 v133, s[40:43], s67 offen offset:1024 lds\n\t"
      "s_cmp_eq_u32 s99, s69\n\t"
      "s_cselect_b32 s67, s68, s99\n\t"
      "s_waitcnt vmcnt(14)\n\t"
      "ds_read_b64_tr_b16 a[32:33], v134 offset:8192\n\t"
      "ds_read_b64_tr_b16 a[34:35], v134 offset:8704\n\t"
      "ds_read_b64_tr_b16 a[36:37], v135 offset:8192\n\t"
      "ds_read_b64_tr_b16 a[38:39], v135 offset:8704\n\t"
      "s_add_u32 m0, s70, 24576\n\t"
      "s_add_u32 s99, s67, 65536\n\t"
      "buffer_load_dwordx4 v132, s[40:43], s67 offen lds\n\t"
      "buffer_load_dwordx4 v133, s[40:43], s67 offen offset:1024 lds\n\t"
      "s_cmp_eq_u32 s99, s69\n\t"
      "s_cselect_b32 s67, s68, s99\n\t"
      "s_waitcnt vmcnt(14)\n\t"
      "s_waitcnt lgkmcnt(4)\n\t"
      "ds_read_b64_tr_b16 a[40:41], v134 offset:10240\n\t"
      "ds_read_b64_tr_b16 a[42:43], v134 offset:10752\n\t"
      "ds_read_b64_tr_b16 a[44:45], v135 offset:10240\n\t"
      "ds_read_b64_tr_b16 a[46:47], v135 offset:10752\n\t"
      "s_add_u32 m0, s70, 26624\n\t"
      "s_add_u32 s99, s67, 65536\n\t"
      "buffer_load_dwordx4 v132, s[40:43], s67 offen lds\n\t"
      "buffer_load_dwordx4 v133, s[40:43], s67 offen offset:1024 lds\n\t"
      "s_cmp_eq_u32 s99, s69\n\t"
      "s_cselect_b32 s67, s68, s99\n\t"
      "s_waitcnt vmcnt(14)\n\t"
      "ds_read_b64_tr_b16 a[48:49], v134 offset:12288\n\t"
      "ds_read_b64_tr_b16 a[50:51], v134 offset:12800\n\t"
      "ds_read_b64_tr_b16 a[52:53], v135 offset:12288\n\t"
      "ds_read_b64_tr_b16 a[54:55], v135 offset:12800\n\t"
      "s_add_u32 m0, s70, 0\n\t"
      "s_add_u32 s99, s67, 65536\n\t"
      "buffer_load_dwordx4 v132, s[40:43], s67 offen lds\n\t"
      "buffer_load_dwordx4 v133, s[40:43], s67 offen offset:1024 lds\n\t"
      "s_cmp_eq_u32 s99, s69\n\t"
      "s_cselect_b32 s67, s68, s99\n\t"
      "s_waitcnt vmcnt(14)\n\t"
      "s_waitcnt lgkmcnt(4)\n\t"
      "ds_read_b64_tr_b16 a[56:57], v134 offset:14336\n\t"
      "ds_read_b64_tr_b16 a[58:59], v134 offset:14848\n\t"
      "ds_read_b64_tr_b16 a[60:61], v135 offset:14336\n\t"
      "ds_read_b64_tr_b16 a[62:63], v135 offset:14848\n\t"
      "s_add_u32 m0, s70, 2048\n\t"
      "s_add_u32 s99, s67, 65536\n\t"
      "buffer_load_dwordx4 v132, s[40:43], s67 offen lds\n\t"
      "buffer_load_dwordx4 v133, s[40:43], s67 offen offset:1024 lds\n\t"
      "s_cmp_eq_u32 s99, s69\n\t"
      "s_cselect_b32 s67, s68, s99\n\t"
      "s_waitcnt vmcnt(14)\n\t"
      "ds_read_b64_tr_b16 a[64:65], v134 offset:16384\n\t"
      "ds_read_b64_tr_b16 a[66:67], v134 offset:16896\n\t"
      "ds_read_b64_tr_b16 a[68:69], v135 offset:16384\n\t"
      "ds_read_b64_tr_b16 a[70:71], v135 offset:16896\n\t"
      "s_add_u32 m0, s70, 4096\n\t"
      "s_add_u32 s99, s67, 65536\n\t"
      "buffer_load_dwordx4 v132, s[40:43], s67 offen lds\n\t"
      "buffer_load_dwordx4 v133, s[40:43], s67 offen offset:1024 lds\n\t"
      "s_cmp_eq_u32 s99, s69\n\t"
      "s_cselect_b32 s67, s68, s99\n\t"
      "s_waitcnt vmcnt(14)\n\t"
      "s_waitcnt lgkmcnt(4)\n\t"
      "ds_read_b64_tr_b16 a[72:73], v134 offset:18432\n\t"
      "ds_read_b64_tr_b16 a[74:75], v134 offset:18944\n\t"
      "ds_read_b64_tr_b16 a[76:77], v135 offset:18432\n\t"
      "ds_read_b64_tr_b16 a[78:79], v135 offset:18944\n\t"
      "s_add_u32 m0, s70, 6144\n\t"
      "s_add_u32 s99, s67, 65536\n\t"
      "buffer_load_dwordx4 v132, s[40:43], s67 offen lds\n\t"
      "buffer_load_dwordx4 v133, s[40:43], s67 offen offset:1024 lds\n\t"
      "s_cmp_eq_u32 s99, s69\n\t"
      "s_cselect_b32 s67, s68, s99\n\t"
      "s_waitcnt vmcnt(14)\n\t"
      "ds_read_b64_tr_b16 a[80:81], v134 offset:20480\n\t"
      "ds_read_b64_tr_b16 a[82:83], v134 offset:20992\n\t"
      "ds_read_b64_tr_b16 a[84:85], v135 offset:20480\n\t"
      "ds_read_b64_tr_b16 a[86:87], v135 offset:20992\n\t"
      "s_add_u32 m0, s70, 8192\n\t"
      "s_add_u32 s99, s67, 65536\n\t"
      "buffer_load_dwordx4 v132, s[40:43], s67 offen lds\n\t"
      "buffer_load_dwordx4 v133, s[40:43], s67 offen offset:1024 lds\n\t"
      "s_cmp_eq_u32 s99, s69\n\t"
      "s_cselect_b32 s67, s68, s99\n\t"
      "s_waitcnt vmcnt(14)\n\t"
      "s_waitcnt lgkmcnt(4)\n\t"
      "ds_read_b64_tr_b16 a[88:89], v134 offset:22528\n\t"
      "ds_read_b64_tr_b16 a[90:91], v134 offset:23040\n\t"
      "ds_read_b64_tr_b16 a[92:93], v135 offset:22528\n\t"
      "ds_read_b64_tr_b16 a[94:95], v135 offset:23040\n\t"
      "s_add_u32 m0, s70, 10240\n\t"
      "s_add_u32 s99, s67, 65536\n\t"
      "buffer_load_dwordx4 v132, s[40:43], s67 offen lds\n\t"
      "buffer_load_dwordx4 v133, s[40:43], s67 offen offset:1024 lds\n\t"
      "s_cmp_eq_u32 s99, s69\n\t"
      "s_cselect_b32 s67, s68, s99\n\t"
      "s_waitcnt vmcnt(14)\n\t"
      "ds_read_b64_tr_b16 a[96:97], v134 offset:24576\n\t"
      "ds_read_b64_tr_b16 a[98:99], v134 offset:25088\n\t"
      "ds_read_b64_tr_b16 a[100:101], v135 offset:24576\n\t"
      "ds_read_b64_tr_b16 a[102:103], v135 offset:25088\n\t"
      "s_add_u32 m0, s70, 12288\n\t"
      "s_add_u32 s99, s67, 65536\n\t"
      "buffer_load_dwordx4 v132, s[40:43], s67 offen lds\n\t"
      "buffer_load_dwordx4 v133, s[40:43], s67 offen offset:1024 lds\n\t"
      "s_cmp_eq_u32 s99, s69\n\t"
      "s_cselect_b32 s67, s68, s99\n\t"
      "s_waitcnt vmcnt(14)\n\t"
      "s_waitcnt lgkmcnt(4)\n\t"
      "ds_read_b64_tr_b16 a[104:105], v134 offset:26624\n\t"
      "ds_read_b64_tr_b16 a[106:107], v134 offset:27136\n\t"
      "ds_read_b64_tr_b16 a[108:109], v135 offset:26624\n\t"
      "ds_read_b64_tr_b16 a[110:111], v135 offset:27136\n\t"
      "s_add_u32 m0, s70, 14336\n\t"
      "s_add_u32 s99, s67, 65536\n\t"
      "buffer_load_dwordx4 v132, s[40:43], s67 offen lds\n\t"
      "buffer_load_dwordx4 v133, s[40:43], s67 offen offset:1024 lds\n\t"
      "s_cmp_eq_u32 s99, s69\n\t"
      "s_cselect_b32 s67, s68, s99\n\t"
      "s_waitcnt vmcnt(14)\n\t"
      "ds_read_b64_tr_b16 a[112:113], v134 offset:0\n\t"
      "ds_read_b64_tr_b16 a[114:115], v134 offset:512\n\t"
      "ds_read_b64_tr_b16 a[116:117], v135 offset:0\n\t"
      "ds_read_b64_tr_b16 a[118:119], v135 offset:512\n\t"
      "s_add_u32 m0, s70, 16384\n\t"
      "s_add_u32 s99, s67, 65536\n\t"
      "buffer_load_dwordx4 v132, s[40:43], s67 offen lds\n\t"
      "buffer_load_dwordx4 v133, s[40:43], s67 offen offset:1024 lds\n\t"
      "s_cmp_eq_u32 s99, s69\n\t"
      "s_cselect_b32 s67, s68, s99\n\t"
      "s_waitcnt vmcnt(14)\n\t"
      "s_waitcnt lgkmcnt(4)\n\t"
      "ds_read_b64_tr_b16 a[120:121], v134 offset:2048\n\t"
      "ds_read_b64_tr_b16 a[122:123], v134 offset:2560\n\t"
      "ds_read_b64_tr_b16 a[124:125], v135 offset:2048\n\t"
      "ds_read_b64_tr_b16 a[126:127], v135 offset:2560\n\t"
      "s_add_u32 m0, s70, 18432\n\t"
      "s_add_u32 s99, s67, 65536\n\t"
      "buffer_load_dwordx4 v132, s[40:43], s67 offen lds\n\t"
      "buffer_load_dwordx4 v133, s[40:43], s67 offen offset:1024 lds\n\t"
      "s_cmp_eq_u32 s99, s69\n\t"
      "s_cselect_b32 s67, s68, s99\n\t"
      "s_waitcnt vmcnt(14)\n\t"
      "ds_read_b64_tr_b16 a[128:129], v134 offset:4096\n\t"
      "ds_read_b64_tr_b16 a[130:131], v134 offset:4608\n\t"
      "ds_read_b64_tr_b16 a[132:133], v135 offset:4096\n\t"
      "ds_read_b64_tr_b16 a[134:135], v135 offset:4608\n\t"
      "s_add_u32 m0, s70, 20480\n\t"
      "s_add_u32 s99, s67, 65536\n\t"
      "buffer_load_dwordx4 v132, s[40:43], s67 offen lds\n\t"
      "buffer_load_dwordx4 v133, s[40:43], s67 offen offset:1024 lds\n\t"
      "s_cmp_eq_u32 s99, s69\n\t"
      "s_cselect_b32 s67, s68, s99\n\t"
      "s_waitcnt vmcnt(14)\n\t"
      "s_waitcnt lgkmcnt(4)\n\t"
      "ds_read_b64_tr_b16 a[136:137], v134 offset:6144\n\t"
      "ds_read_b64_tr_b16 a[138:139], v134 offset:6656\n\t"
      "ds_read_b64_tr_b16 a[140:141], v135 offset:6144\n\t"
      "ds_read_b64_tr_b16 a[142:143], v135 offset:6656\n\t"
      "s_add_u32 m0, s70, 22528\n\t"
      "s_add_u32 s99, s67, 65536\n\t"
      "buffer_load_dwordx4 v132, s[40:43], s67 offen lds\n\t"
      "buffer_load_dwordx4 v133, s[40:43], s67 offen offset:1024 lds\n\t"
      "s_cmp_eq_u32 s99, s69\n\t"
      "s_cselect_b32 s67, s68, s99\n\t"
      "s_waitcnt vmcnt(14)\n\t"
      "ds_read_b64_tr_b16 a[144:145], v134 offset:8192\n\t"
      "ds_read_b64_tr_b16 a[146:147], v134 offset:8704\n\t"
      "ds_read_b64_tr_b16 a[148:149], v135 offset:8192\n\t"
      "ds_read_b64_tr_b16 a[150:151], v135 offset:8704\n\t"
      "s_add_u32 m0, s70, 24576\n\t"
      "s_add_u32 s99, s67, 65536\n\t"
      "buffer_load_dwordx4 v132, s[40:43], s67 offen lds\n\t"
      "buffer_load_dwordx4 v133, s[40:43], s67 offen offset:1024 lds\n\t"
      "s_cmp_eq_u32 s99, s69\n\t"
      "s_cselect_b32 s67, s68, s99\n\t"
      "s_waitcnt vmcnt(14)\n\t"
      "s_waitcnt lgkmcnt(4)\n\t"
      "ds_read_b64_tr_b16 a[152:153], v134 offset:10240\n\t"
      "ds_read_b64_tr_b16 a[154:155], v134 offset:10752\n\t"
      "ds_read_b64_tr_b16 a[156:157], v135 offset:10240\n\t"
      "ds_read_b64_tr_b16 a[158:159], v135 offset:10752\n\t"
      "s_add_u32 m0, s70, 26624\n\t"
      "s_add_u32 s99, s67, 65536\n\t"
      "buffer_load_dwordx4 v132, s[40:43], s67 offen lds\n\t"
      "buffer_load_dwordx4 v133, s[40:43], s67 offen offset:1024 lds\n\t"
      "s_cmp_eq_u32 s99, s69\n\t"
      "s_cselect_b32 s67, s68, s99\n\t"
      "s_waitcnt vmcnt(14)\n\t"
      "ds_read_b64_tr_b16 a[160:161], v134 offset:12288\n\t"
      "ds_read_b64_tr_b16 a[162:163], v134 offset:12800\n\t"
      "ds_read_b64_tr_b16 a[164:165], v135 offset:12288\n\t"
      "ds_read_b64_tr_b16 a[166:167], v135 offset:12800\n\t"
      "s_waitcnt vmcnt(12)\n\t"
      "s_waitcnt lgkmcnt(4)\n\t"
      "ds_read_b64_tr_b16 a[168:169], v134 offset:14336\n\t"
      "ds_read_b64_tr_b16 a[170:171], v134 offset:14848\n\t"
      "ds_read_b64_tr_b16 a[172:173], v135 offset:14336\n\t"
      "ds_read_b64_tr_b16 a[174:175], v135 offset:14848\n\t"
      "s_waitcnt vmcnt(10)\n\t"
      "ds_read_b64_tr_b16 a[176:177], v134 offset:16384\n\t"
      "ds_read_b64_tr_b16 a[178:179], v134 offset:16896\n\t"
      "ds_read_b64_tr_b16 a[180:181], v135 offset:16384\n\t"
      "ds_read_b64_tr_b16 a[182:183], v135 offset:16896\n\t"
      "s_waitcnt vmcnt(8)\n\t"
      "s_waitcnt lgkmcnt(4)\n\t"
      "ds_read_b64_tr_b16 a[184:185], v134 offset:18432\n\t"
      "ds_read_b64_tr_b16 a[186:187], v134 offset:18944\n\t"
      "ds_read_b64_tr_b16 a[188:189], v135 offset:18432\n\t"
      "ds_read_b64_tr_b16 a[190:191], v135 offset:18944\n\t"
      "s_waitcnt vmcnt(6)\n\t"
      "ds_read_b64_tr_b16 a[192:193], v134 offset:20480\n\t"
      "ds_read_b64_tr_b16 a[194:195], v134 offset:20992\n\t"
      "ds_read_b64_tr_b16 a[196:197], v135 offset:20480\n\t"
      "ds_read_b64_tr_b16 a[198:199], v135 offset:20992\n\t"
      "s_waitcnt vmcnt(4)\n\t"
      "s_waitcnt lgkmcnt(4)\n\t"
      "ds_read_b64_tr_b16 a[200:201], v134 offset:22528\n\t"
      "ds_read_b64_tr_b16 a[202:203], v134 offset:23040\n\t"
      "ds_read_b64_tr_b16 a[204:205], v135 offset:22528\n\t"
      "ds_read_b64_tr_b16 a[206:207], v135 offset:23040\n\t"
      "s_waitcnt vmcnt(2)\n\t"
      "ds_read_b64_tr_b16 a[208:209], v134 offset:24576\n\t"
      "ds_read_b64_tr_b16 a[210:211], v134 offset:25088\n\t"
      "ds_read_b64_tr_b16 a[212:213], v135 offset:24576\n\t"
      "ds_read_b64_tr_b16 a[214:215], v135 offset:25088\n\t"
      "s_waitcnt vmcnt(0)\n\t"
      "s_waitcnt lgkmcnt(4)\n\t"
      "ds_read_b64_tr_b16 a[216:217], v134 offset:26624\n\t"
      "ds_read_b64_tr_b16 a[218:219], v134 offset:27136\n\t"
      "ds_read_b64_tr_b16 a[220:221], v135 offset:26624\n\t"
      "ds_read_b64_tr_b16 a[222:223], v135 offset:27136\n\t"
      "s_waitcnt lgkmcnt(0)\n\t"
      "Lgate11_%=:\n\t"
      "s_sleep 4\n\t"
      "s_memrealtime s[96:97]\n\t"
      "s_waitcnt lgkmcnt(0)\n\t"
      "s_sub_u32 s96, s96, s98\n\t"
      "s_cmp_lt_u32 s96, 850\n\t"
      "s_cbranch_scc1 Lgate11_%=\n\t"
      "Lw2done%=:\n\t"
      "s_lshl_b32 s72, s63, 5\n\t"
      "s_mul_i32 s73, s56, 7\n\t"
      "s_add_u32 s72, s72, s73\n\t"
      "s_lshl_b32 s72, s72, 7\n\t"
      "s_add_u32 s72, s72, 36864\n\t"
      "v_min_u32 v198, 6, v128\n\t"
      "v_lshlrev_b32 v198, 7, v198\n\t"
      "v_add_u32 v198, s72, v198\n\t"
      "s_mul_i32 s92, s63, 112\n\t"
      "s_mul_i32 s72, s56, 28\n\t"
      "s_add_u32 s92, s92, s72\n\t"
      "s_lshl_b32 s92, s92, 11\n\t"
      "s_add_u32 s93, s92, 57344\n\t"
      "s_mul_i32 s72, s63, 7\n\t"
      "s_lshr_b32 s72, s72, 1\n\t"
      "s_lshl_b32 s72, s72, 11\n\t"
      "s_add_u32 s91, s92, s72\n\t"
      "v_lshlrev_b32 v148, 16, v148\n\t"
      "s_mov_b32 s94, 0x2000\n\t"
      "Lpoll12_%=:\n\t"
      "global_load_dwordx2 v[200:201], v198, s[34:35] sc1\n\t"
      "s_waitcnt vmcnt(0)\n\t"
      "v_cmp_eq_u64 vcc, v[200:201], v[138:139]\n\t"
      "s_nop 1\n\t"
      "s_cmp_eq_u64 vcc, -1\n\t"
      "s_cbranch_scc1 Lpollok13_%=\n\t"
      "s_sleep 1\n\t"
      "s_sub_u32 s94, s94, 1\n\t"
      "s_cbranch_scc0 Lpoll12_%=\n\t"
      "Lpollok13_%=:\n\t"
      "buffer_load_dwordx4 v[64:67], v136, s[48:51], s91 offen sc1\n\t"
      "buffer_load_dwordx4 v[68:71], v137, s[48:51], s91 offen sc1\n\t"
      "s_add_u32 s91, s91, 2048\n\t"
      "s_cmp_eq_u32 s91, s93\n\t"
      "s_cselect_b32 s91, s92, s91\n\t"
      "buffer_load_dwordx4 v[72:75], v136, s[48:51], s91 offen sc1\n\t"
      "buffer_load_dwordx4 v[76:79], v137, s[48:51], s91 offen sc1\n\t"
      "s_add_u32 s91, s91, 2048\n\t"
      "s_cmp_eq_u32 s91, s93\n\t"
      "s_cselect_b32 s91, s92, s91\n\t"
      "buffer_load_dwordx4 v[80:83], v136, s[48:51], s91 offen sc1\n\t"
      "buffer_load_dwordx4 v[84:87], v137, s[48:51], s91 offen sc1\n\t"
      "s_add_u32 s91, s91, 2048\n\t"
      "s_cmp_eq_u32 s91, s93\n\t"
      "s_cselect_b32 s91, s92, s91\n\t"
      "buffer_load_dwordx4 v[88:91], v136, s[48:51], s91 offen sc1\n\t"
      "buffer_load_dwordx4 v[92:95], v137, s[48:51], s91 offen sc1\n\t"
      "s_add_u32 s91, s91, 2048\n\t"
      "s_cmp_eq_u32 s91, s93\n\t"
      "s_cselect_b32 s91, s92, s91\n\t"
      "buffer_load_dwordx4 v[96:99], v136, s[48:51], s91 offen sc1\n\t"
      "buffer_load_dwordx4 v[100:103], v137, s[48:51], s91 offen sc1\n\t"
      "s_add_u32 s91, s91, 2048\n\t"
      "s_cmp_eq_u32 s91, s93\n\t"
      "s_cselect_b32 s91, s92, s91\n\t"
      "buffer_load_dwordx4 v[104:107], v136, s[48:51], s91 offen sc1\n\t"
      "buffer_load_dwordx4 v[108:111], v137, s[48:51], s91 offen sc1\n\t"
      "s_add_u32 s91, s91, 2048\n\t"
      "s_cmp_eq_u32 s91, s93\n\t"
      "s_cselect_b32 s91, s92, s91\n\t"
      "buffer_load_dwordx4 v[112:115], v136, s[48:51], s91 offen sc1\n\t"
      "buffer_load_dwordx4 v[116:119], v137, s[48:51], s91 offen sc1\n\t"
      "s_add_u32 s91, s91, 2048\n\t"
      "s_cmp_eq_u32 s91, s93\n\t"
      "s_cselect_b32 s91, s92, s91\n\t"
      "buffer_load_dwordx4 v[120:123], v136, s[48:51], s91 offen sc1\n\t"
      "buffer_load_dwordx4 v[124:127], v137, s[48:51], s91 offen sc1\n\t"
      "s_add_u32 s91, s91, 2048\n\t"
      "s_cmp_eq_u32 s91, s93\n\t"
      "s_cselect_b32 s91, s92, s91\n\t"
      "s_waitcnt vmcnt(15)\n\t"
      "v_mfma_f32_32x32x16_bf16 v[0:15], v[64:67], a[0:3], 0\n\t"
      "v_mfma_f32_32x32x16_bf16 v[32:47], v[64:67], a[4:7], 0\n\t"
      "buffer_load_dwordx4 v[64:67], v136, s[48:51], s91 offen sc1\n\t"
      "s_waitcnt vmcnt(15)\n\t"
      "v_mfma_f32_32x32x16_bf16 v[16:31], v[68:71], a[0:3], 0\n\t"
      "v_mfma_f32_32x32x16_bf16 v[48:63], v[68:71], a[4:7], 0\n\t"
      "buffer_load_dwordx4 v[68:71], v137, s[48:51], s91 offen sc1\n\t"
      "s_add_u32 s91, s91, 2048\n\t"
      "s_cmp_eq_u32 s91, s93\n\t"
      "s_cselect_b32 s91, s92, s91\n\t"
      "s_waitcnt vmcnt(15)\n\t"
      "v_mfma_f32_32x32x16_bf16 v[0:15], v[72:75], a[8:11], v[0:15]\n\t"
      "v_mfma_f32_32x32x16_bf16 v[32:47], v[72:75], a[12:15], v[32:47]\n\t"
      "buffer_load_dwordx4 v[72:75], v136, s[48:51], s91 offen sc1\n\t"
      "s_waitcnt vmcnt(15)\n\t"
      "v_mfma_f32_32x32x16_bf16 v[16:31], v[76:79], a[8:11], v[16:31]\n\t"
      "v_mfma_f32_32x32x16_bf16 v[48:63], v[76:79], a[12:15], v[48:63]\n\t"
      "buffer_load_dwordx4 v[76:79], v137, s[48:51], s91 offen sc1\n\t"
      "s_add_u32 s91, s91, 2048\n\t"
      "s_cmp_eq_u32 s91, s93\n\t"
      "s_cselect_b32 s91, s92, s91\n\t"
      "s_waitcnt vmcnt(15)\n\t"
      "v_mfma_f32_32x32x16_bf16 v[0:15], v[80:83], a[16:19], v[0:15]\n\t"
      "v_mfma_f32_32x32x16_bf16 v[32:47], v[80:83], a[20:23], v[32:47]\n\t"
      "buffer_load_dwordx4 v[80:83], v136, s[48:51], s91 offen sc1\n\t"
      "s_waitcnt vmcnt(15)\n\t"
      "v_mfma_f32_32x32x16_bf16 v[16:31], v[84:87], a[16:19], v[16:31]\n\t"
      "v_mfma_f32_32x32x16_bf16 v[48:63], v[84:87], a[20:23], v[48:63]\n\t"
      "buffer_load_dwordx4 v[84:87], v137, s[48:51], s91 offen sc1\n\t"
      "s_add_u32 s91, s91, 2048\n\t"
      "s_cmp_eq_u32 s91, s93\n\t"
      "s_cselect_b32 s91, s92, s91\n\t"
      "s_waitcnt vmcnt(15)\n\t"
      "v_mfma_f32_32x32x16_bf16 v[0:15], v[88:91], a[24:27], v[0:15]\n\t"
      "v_mfma_f32_32x32x16_bf16 v[32:47], v[88:91], a[28:31], v[32:47]\n\t"
      "buffer_load_dwordx4 v[88:91], v136, s[48:51], s91 offen sc1\n\t"
      "s_waitcnt vmcnt(15)\n\t"
      "v_mfma_f32_32x32x16_bf16 v[16:31], v[92:95], a[24:27], v[16:31]\n\t"
      "v_mfma_f32_32x32x16_bf16 v[48:63], v[92:95], a[28:31], v[48:63]\n\t"
      "buffer_load_dwordx4 v[92:95], v137, s[48:51], s91 offen sc1\n\t"
      "s_add_u32 s91, s91, 2048\n\t"
      "s_cmp_eq_u32 s91, s93\n\t"
      "s_cselect_b32 s91, s92, s91\n\t"
      "s_waitcnt vmcnt(15)\n\t"
      "v_mfma_f32_32x32x16_bf16 v[0:15], v[96:99], a[32:35], v[0:15]\n\t"
      "v_mfma_f32_32x32x16_bf16 v[32:47], v[96:99], a[36:39], v[32:47]\n\t"
      "buffer_load_dwordx4 v[96:99], v136, s[48:51], s91 offen sc1\n\t"
      "s_waitcnt vmcnt(15)\n\t"
      "v_mfma_f32_32x32x16_bf16 v[16:31], v[100:103], a[32:35], v[16:31]\n\t"
      "v_mfma_f32_32x32x16_bf16 v[48:63], v[100:103], a[36:39], v[48:63]\n\t"
      "buffer_load_dwordx4 v[100:103], v137, s[48:51], s91 offen sc1\n\t"
      "s_add_u32 s91, s91, 2048\n\t"
      "s_cmp_eq_u32 s91, s93\n\t"
      "s_cselect_b32 s91, s92, s91\n\t"
      "s_waitcnt vmcnt(15)\n\t"
      "v_mfma_f32_32x32x16_bf16 v[0:15], v[104:107], a[40:43], v[0:15]\n\t"
      "v_mfma_f32_32x32x16_bf16 v[32:47], v[104:107], a[44:47], v[32:47]\n\t"
      "buffer_load_dwordx4 v[104:107], v136, s[48:51], s91 offen sc1\n\t"
      "s_waitcnt vmcnt(15)\n\t"
      "v_mfma_f32_32x32x16_bf16 v[16:31], v[108:111], a[40:43], v[16:31]\n\t"
      "v_mfma_f32_32x32x16_bf16 v[48:63], v[108:111], a[44:47], v[48:63]\n\t"
      "buffer_load_dwordx4 v[108:111], v137, s[48:51], s91 offen sc1\n\t"
      "s_add_u32 s91, s91, 2048\n\t"
      "s_cmp_eq_u32 s91, s93\n\t"
      "s_cselect_b32 s91, s92, s91\n\t"
      "s_waitcnt vmcnt(15)\n\t"
      "v_mfma_f32_32x32x16_bf16 v[0:15], v[112:115], a[48:51], v[0:15]\n\t"
      "v_mfma_f32_32x32x16_bf16 v[32:47], v[112:115], a[52:55], v[32:47]\n\t"
      "buffer_load_dwordx4 v[112:115], v136, s[48:51], s91 offen sc1\n\t"
      "s_waitcnt vmcnt(15)\n\t"
      "v_mfma_f32_32x32x16_bf16 v[16:31], v[116:119], a[48:51], v[16:31]\n\t"
      "v_mfma_f32_32x32x16_bf16 v[48:63], v[116:119], a[52:55], v[48:63]\n\t"
      "buffer_load_dwordx4 v[116:119], v137, s[48:51], s91 offen sc1\n\t"
      "s_add_u32 s91, s91, 2048\n\t"
      "s_cmp_eq_u32 s91, s93\n\t"
      "s_cselect_b32 s91, s92, s91\n\t"
      "s_waitcnt vmcnt(15)\n\t"
      "v_mfma_f32_32x32x16_bf16 v[0:15], v[120:123], a[56:59], v[0:15]\n\t"
      "v_mfma_f32_32x32x16_bf16 v[32:47], v[120:123], a[60:63], v[32:47]\n\t"
      "buffer_load_dwordx4 v[120:123], v136, s[48:51], s91 offen sc1\n\t"
      "s_waitcnt vmcnt(15)\n\t"
      "v_mfma_f32_32x32x16_bf16 v[16:31], v[124:127], a[56:59], v[16:31]\n\t"
      "v_mfma_f32_32x32x16_bf16 v[48:63], v[124:127], a[60:63], v[48:63]\n\t"
      "buffer_load_dwordx4 v[124:127], v137, s[48:51], s91 offen sc1\n\t"
      "s_add_u32 s91, s91, 2048\n\t"
      "s_cmp_eq_u32 s91, s93\n\t"
      "s_cselect_b32 s91, s92, s91\n\t"
      "s_waitcnt vmcnt(15)\n\t"
      "v_mfma_f32_32x32x16_bf16 v[0:15], v[64:67], a[64:67], v[0:15]\n\t"
      "v_mfma_f32_32x32x16_bf16 v[32:47], v[64:67], a[68:71], v[32:47]\n\t"
      "buffer_load_dwordx4 v[64:67], v136, s[48:51], s91 offen sc1\n\t"
      "s_waitcnt vmcnt(15)\n\t"
      "v_mfma_f32_32x32x16_bf16 v[16:31], v[68:71], a[64:67], v[16:31]\n\t"
      "v_mfma_f32_32x32x16_bf16 v[48:63], v[68:71], a[68:71], v[48:63]\n\t"
      "buffer_load_dwordx4 v[68:71], v137, s[48:51], s91 offen sc1\n\t"
      "s_add_u32 s91, s91, 2048\n\t"
      "s_cmp_eq_u32 s91, s93\n\t"
      "s_cselect_b32 s91, s92, s91\n\t"
      "s_waitcnt vmcnt(15)\n\t"
      "v_mfma_f32_32x32x16_bf16 v[0:15], v[72:75], a[72:75], v[0:15]\n\t"
      "v_mfma_f32_32x32x16_bf16 v[32:47], v[72:75], a[76:79], v[32:47]\n\t"
      "buffer_load_dwordx4 v[72:75], v136, s[48:51], s91 offen sc1\n\t"
      "s_waitcnt vmcnt(15)\n\t"
      "v_mfma_f32_32x32x16_bf16 v[16:31], v[76:79], a[72:75], v[16:31]\n\t"
      "v_mfma_f32_32x32x16_bf16 v[48:63], v[76:79], a[76:79], v[48:63]\n\t"
      "buffer_load_dwordx4 v[76:79], v137, s[48:51], s91 offen sc1\n\t"
      "s_add_u32 s91, s91, 2048\n\t"
      "s_cmp_eq_u32 s91, s93\n\t"
      "s_cselect_b32 s91, s92, s91\n\t"
      "s_waitcnt vmcnt(15)\n\t"
      "v_mfma_f32_32x32x16_bf16 v[0:15], v[80:83], a[80:83], v[0:15]\n\t"
      "v_mfma_f32_32x32x16_bf16 v[32:47], v[80:83], a[84:87], v[32:47]\n\t"
      "buffer_load_dwordx4 v[80:83], v136, s[48:51], s91 offen sc1\n\t"
      "s_waitcnt vmcnt(15)\n\t"
      "v_mfma_f32_32x32x16_bf16 v[16:31], v[84:87], a[80:83], v[16:31]\n\t"
      "v_mfma_f32_32x32x16_bf16 v[48:63], v[84:87], a[84:87], v[48:63]\n\t"
      "buffer_load_dwordx4 v[84:87], v137, s[48:51], s91 offen sc1\n\t"
      "s_add_u32 s91, s91, 2048\n\t"
      "s_cmp_eq_u32 s91, s93\n\t"
      "s_cselect_b32 s91, s92, s91\n\t"
      "s_waitcnt vmcnt(15)\n\t"
      "v_mfma_f32_32x32x16_bf16 v[0:15], v[88:91], a[88:91], v[0:15]\n\t"
      "v_mfma_f32_32x32x16_bf16 v[32:47], v[88:91], a[92:95], v[32:47]\n\t"
      "buffer_load_dwordx4 v[88:91], v136, s[48:51], s91 offen sc1\n\t"
      "s_waitcnt vmcnt(15)\n\t"
      "v_mfma_f32_32x32x16_bf16 v[16:31], v[92:95], a[88:91], v[16:31]\n\t"
      "v_mfma_f32_32x32x16_bf16 v[48:63], v[92:95], a[92:95], v[48:63]\n\t"
      "buffer_load_dwordx4 v[92:95], v137, s[48:51], s91 offen sc1\n\t"
      "s_add_u32 s91, s91, 2048\n\t"
      "s_cmp_eq_u32 s91, s93\n\t"
      "s_cselect_b32 s91, s92, s91\n\t"
      "s_waitcnt vmcnt(15)\n\t"
      "v_mfma_f32_32x32x16_bf16 v[0:15], v[96:99], a[96:99], v[0:15]\n\t"
      "v_mfma_f32_32x32x16_bf16 v[32:47], v[96:99], a[100:103], v[32:47]\n\t"
      "buffer_load_dwordx4 v[96:99], v136, s[48:51], s91 offen sc1\n\t"
      "s_waitcnt vmcnt(15)\n\t"
      "v_mfma_f32_32x32x16_bf16 v[16:31], v[100:103], a[96:99], v[16:31]\n\t"
      "v_mfma_f32_32x32x16_bf16 v[48:63], v[100:103], a[100:103], v[48:63]\n\t"
      "buffer_load_dwordx4 v[100:103], v137, s[48:51], s91 offen sc1\n\t"
      "s_add_u32 s91, s91, 2048\n\t"
      "s_cmp_eq_u32 s91, s93\n\t"
      "s_cselect_b32 s91, s92, s91\n\t"
      "s_waitcnt vmcnt(15)\n\t"
      "v_mfma_f32_32x32x16_bf16 v[0:15], v[104:107], a[104:107], v[0:15]\n\t"
      "v_mfma_f32_32x32x16_bf16 v[32:47], v[104:107], a[108:111], v[32:47]\n\t"
      "buffer_load_dwordx4 v[104:107], v136, s[48:51], s91 offen sc1\n\t"
      "s_waitcnt vmcnt(15)\n\t"
      "v_mfma_f32_32x32x16_bf16 v[16:31], v[108:111], a[104:107], v[16:31]\n\t"
      "v_mfma_f32_32x32x16_bf16 v[48:63], v[108:111], a[108:111], v[48:63]\n\t"
      "buffer_load_dwordx4 v[108:111], v137, s[48:51], s91 offen sc1\n\t"
      "s_add_u32 s91, s91, 2048\n\t"
      "s_cmp_eq_u32 s91, s93\n\t"
      "s_cselect_b32 s91, s92, s91\n\t"
      "s_waitcnt vmcnt(15)\n\t"
      "v_mfma_f32_32x32x16_bf16 v[0:15], v[112:115], a[112:115], v[0:15]\n\t"
      "v_mfma_f32_32x32x16_bf16 v[32:47], v[112:115], a[116:119], v[32:47]\n\t"
      "buffer_load_dwordx4 v[112:115], v136, s[48:51], s91 offen sc1\n\t"
      "s_waitcnt vmcnt(15)\n\t"
      "v_mfma_f32_32x32x16_bf16 v[16:31], v[116:119], a[112:115], v[16:31]\n\t"
      "v_mfma_f32_32x32x16_bf16 v[48:63], v[116:119], a[116:119], v[48:63]\n\t"
      "buffer_load_dwordx4 v[116:119], v137, s[48:51], s91 offen sc1\n\t"
      "s_add_u32 s91, s91, 2048\n\t"
      "s_cmp_eq_u32 s91, s93\n\t"
      "s_cselect_b32 s91, s92, s91\n\t"
      "s_waitcnt vmcnt(15)\n\t"
      "v_mfma_f32_32x32x16_bf16 v[0:15], v[120:123], a[120:123], v[0:15]\n\t"
      "v_mfma_f32_32x32x16_bf16 v[32:47], v[120:123], a[124:127], v[32:47]\n\t"
      "buffer_load_dwordx4 v[120:123], v136, s[48:51], s91 offen sc1\n\t"
      "s_waitcnt vmcnt(15)\n\t"
      "v_mfma_f32_32x32x16_bf16 v[16:31], v[124:127], a[120:123], v[16:31]\n\t"
      "v_mfma_f32_32x32x16_bf16 v[48:63], v[124:127], a[124:127], v[48:63]\n\t"
      "buffer_load_dwordx4 v[124:127], v137, s[48:51], s91 offen sc1\n\t"
      "s_add_u32 s91, s91, 2048\n\t"
      "s_cmp_eq_u32 s91, s93\n\t"
      "s_cselect_b32 s91, s92, s91\n\t"
      "s_waitcnt vmcnt(15)\n\t"
      "v_mfma_f32_32x32x16_bf16 v[0:15], v[64:67], a[128:131], v[0:15]\n\t"
      "v_mfma_f32_32x32x16_bf16 v[32:47], v[64:67], a[132:135], v[32:47]\n\t"
      "buffer_load_dwordx4 v[64:67], v136, s[48:51], s91 offen sc1\n\t"
      "s_waitcnt vmcnt(15)\n\t"
      "v_mfma_f32_32x32x16_bf16 v[16:31], v[68:71], a[128:131], v[16:31]\n\t"
      "v_mfma_f32_32x32x16_bf16 v[48:63], v[68:71], a[132:135], v[48:63]\n\t"
      "buffer_load_dwordx4 v[68:71], v137, s[48:51], s91 offen sc1\n\t"
      "s_add_u32 s91, s91, 2048\n\t"
      "s_cmp_eq_u32 s91, s93\n\t"
      "s_cselect_b32 s91, s92, s91\n\t"
      "s_waitcnt vmcnt(15)\n\t"
      "v_mfma_f32_32x32x16_bf16 v[0:15], v[72:75], a[136:139], v[0:15]\n\t"
      "v_mfma_f32_32x32x16_bf16 v[32:47], v[72:75], a[140:143], v[32:47]\n\t"
      "buffer_load_dwordx4 v[72:75], v136, s[48:51], s91 offen sc1\n\t"
      "s_waitcnt vmcnt(15)\n\t"
      "v_mfma_f32_32x32x16_bf16 v[16:31], v[76:79], a[136:139], v[16:31]\n\t"
      "v_mfma_f32_32x32x16_bf16 v[48:63], v[76:79], a[140:143], v[48:63]\n\t"
      "buffer_load_dwordx4 v[76:79], v137, s[48:51], s91 offen sc1\n\t"
      "s_add_u32 s91, s91, 2048\n\t"
      "s_cmp_eq_u32 s91, s93\n\t"
      "s_cselect_b32 s91, s92, s91\n\t"
      "s_waitcnt vmcnt(15)\n\t"
      "v_mfma_f32_32x32x16_bf16 v[0:15], v[80:83], a[144:147], v[0:15]\n\t"
      "v_mfma_f32_32x32x16_bf16 v[32:47], v[80:83], a[148:151], v[32:47]\n\t"
      "buffer_load_dwordx4 v[80:83], v136, s[48:51], s91 offen sc1\n\t"
      "s_waitcnt vmcnt(15)\n\t"
      "v_mfma_f32_32x32x16_bf16 v[16:31], v[84:87], a[144:147], v[16:31]\n\t"
      "v_mfma_f32_32x32x16_bf16 v[48:63], v[84:87], a[148:151], v[48:63]\n\t"
      "buffer_load_dwordx4 v[84:87], v137, s[48:51], s91 offen sc1\n\t"
      "s_add_u32 s91, s91, 2048\n\t"
      "s_cmp_eq_u32 s91, s93\n\t"
      "s_cselect_b32 s91, s92, s91\n\t"
      "s_waitcnt vmcnt(15)\n\t"
      "v_mfma_f32_32x32x16_bf16 v[0:15], v[88:91], a[152:155], v[0:15]\n\t"
      "v_mfma_f32_32x32x16_bf16 v[32:47], v[88:91], a[156:159], v[32:47]\n\t"
      "buffer_load_dwordx4 v[88:91], v136, s[48:51], s91 offen sc1\n\t"
      "s_waitcnt vmcnt(15)\n\t"
      "v_mfma_f32_32x32x16_bf16 v[16:31], v[92:95], a[152:155], v[16:31]\n\t"
      "v_mfma_f32_32x32x16_bf16 v[48:63], v[92:95], a[156:159], v[48:63]\n\t"
      "buffer_load_dwordx4 v[92:95], v137, s[48:51], s91 offen sc1\n\t"
      "s_add_u32 s91, s91, 2048\n\t"
      "s_cmp_eq_u32 s91, s93\n\t"
      "s_cselect_b32 s91, s92, s91\n\t"
      "s_waitcnt vmcnt(15)\n\t"
      "v_mfma_f32_32x32x16_bf16 v[0:15], v[96:99], a[160:163], v[0:15]\n\t"
      "v_mfma_f32_32x32x16_bf16 v[32:47], v[96:99], a[164:167], v[32:47]\n\t"
      "s_waitcnt vmcnt(14)\n\t"
      "v_mfma_f32_32x32x16_bf16 v[16:31], v[100:103], a[160:163], v[16:31]\n\t"
      "v_mfma_f32_32x32x16_bf16 v[48:63], v[100:103], a[164:167], v[48:63]\n\t"
      "s_waitcnt vmcnt(13)\n\t"
      "v_mfma_f32_32x32x16_bf16 v[0:15], v[104:107], a[168:171], v[0:15]\n\t"
      "v_mfma_f32_32x32x16_bf16 v[32:47], v[104:107], a[172:175], v[32:47]\n\t"
      "s_waitcnt vmcnt(12)\n\t"
      "v_mfma_f32_32x32x16_bf16 v[16:31], v[108:111], a[168:171], v[16:31]\n\t"
      "v_mfma_f32_32x32x16_bf16 v[48:63], v[108:111], a[172:175], v[48:63]\n\t"
      "s_waitcnt vmcnt(11)\n\t"
      "v_mfma_f32_32x32x16_bf16 v[0:15], v[112:115], a[176:179], v[0:15]\n\t"
      "v_mfma_f32_32x32x16_bf16 v[32:47], v[112:115], a[180:183], v[32:47]\n\t"
      "s_waitcnt vmcnt(10)\n\t"
      "v_mfma_f32_32x32x16_bf16 v[16:31], v[116:119], a[176:179], v[16:31]\n\t"
      "v_mfma_f32_32x32x16_bf16 v[48:63], v[116:119], a[180:183], v[48:63]\n\t"
      "s_waitcnt vmcnt(9)\n\t"
      "v_mfma_f32_32x32x16_bf16 v[0:15], v[120:123], a[184:187], v[0:15]\n\t"
      "v_mfma_f32_32x32x16_bf16 v[32:47], v[120:123], a[188:191], v[32:47]\n\t"
      "s_waitcnt vmcnt(8)\n\t"
      "v_mfma_f32_32x32x16_bf16 v[16:31], v[124:127], a[184:187], v[16:31]\n\t"
      "v_mfma_f32_32x32x16_bf16 v[48:63], v[124:127], a[188:191], v[48:63]\n\t"
      "s_waitcnt vmcnt(7)\n\t"
      "v_mfma_f32_32x32x16_bf16 v[0:15], v[64:67], a[192:195], v[0:15]\n\t"
      "v_mfma_f32_32x32x16_bf16 v[32:47], v[64:67], a[196:199], v[32:47]\n\t"
      "s_waitcnt vmcnt(6)\n\t"
      "v_mfma_f32_32x32x16_bf16 v[16:31], v[68:71], a[192:195], v[16:31]\n\t"
      "v_mfma_f32_32x32x16_bf16 v[48:63], v[68:71], a[196:199], v[48:63]\n\t"
      "s_waitcnt vmcnt(5)\n\t"
      "v_mfma_f32_32x32x16_bf16 v[0:15], v[72:75], a[200:203], v[0:15]\n\t"
      "v_mfma_f32_32x32x16_bf16 v[32:47], v[72:75], a[204:207], v[32:47]\n\t"
      "s_waitcnt vmcnt(4)\n\t"
      "v_mfma_f32_32x32x16_bf16 v[16:31], v[76:79], a[200:203], v[16:31]\n\t"
      "v_mfma_f32_32x32x16_bf16 v[48:63], v[76:79], a[204:207], v[48:63]\n\t"
      "s_waitcnt vmcnt(3)\n\t"
      "v_mfma_f32_32x32x16_bf16 v[0:15], v[80:83], a[208:211], v[0:15]\n\t"
      "v_mfma_f32_32x32x16_bf16 v[32:47], v[80:83], a[212:215], v[32:47]\n\t"
      "s_waitcnt vmcnt(2)\n\t"
      "v_mfma_f32_32x32x16_bf16 v[16:31], v[84:87], a[208:211], v[16:31]\n\t"
      "v_mfma_f32_32x32x16_bf16 v[48:63], v[84:87], a[212:215], v[48:63]\n\t"
      "s_waitcnt vmcnt(1)\n\t"
      "v_mfma_f32_32x32x16_bf16 v[0:15], v[88:91], a[216:219], v[0:15]\n\t"
      "v_mfma_f32_32x32x16_bf16 v[32:47], v[88:91], a[220:223], v[32:47]\n\t"
      "s_waitcnt vmcnt(0)\n\t"
      "v_mfma_f32_32x32x16_bf16 v[16:31], v[92:95], a[216:219], v[16:31]\n\t"
      "v_mfma_f32_32x32x16_bf16 v[48:63], v[92:95], a[220:223], v[48:63]\n\t"
      "s_nop 7\n\t"
      "s_nop 3\n\t"
      "ds_write_b128 v150, v[32:35] offset:0\n\t"
      "ds_write_b128 v150, v[36:39] offset:1024\n\t"
      "ds_write_b128 v150, v[40:43] offset:2048\n\t"
      "ds_write_b128 v150, v[44:47] offset:3072\n\t"
      "ds_write_b128 v149, v[16:19] offset:0\n\t"
      "ds_write_b128 v149, v[20:23] offset:1024\n\t"
      "ds_write_b128 v149, v[24:27] offset:2048\n\t"
      "ds_write_b128 v149, v[28:31] offset:3072\n\t"
      "ds_write_b128 v151, v[48:51] offset:0\n\t"
      "ds_write_b128 v151, v[52:55] offset:1024\n\t"
      "ds_write_b128 v151, v[56:59] offset:2048\n\t"
      "ds_write_b128 v151, v[60:63] offset:3072\n\t"
      "s_waitcnt lgkmcnt(0)\n\t"
      "s_barrier\n\t"
      "ds_read_b128 v[64:67], v152 offset:0\n\t"
      "ds_read_b128 v[68:71], v152 offset:1024\n\t"
      "ds_read_b128 v[72:75], v152 offset:2048\n\t"
      "ds_read_b128 v[76:79], v152 offset:3072\n\t"
      "ds_read_b128 v[80:83], v152 offset:4096\n\t"
      "ds_read_b128 v[84:87], v152 offset:5120\n\t"
      "ds_read_b128 v[88:91], v152 offset:6144\n\t"
      "ds_read_b128 v[92:95], v152 offset:7168\n\t"
      "ds_read_b128 v[96:99], v152 offset:8192\n\t"
      "ds_read_b128 v[100:103], v152 offset:9216\n\t"
      "ds_read_b128 v[104:107], v152 offset:10240\n\t"
      "ds_read_b128 v[108:111], v152 offset:11264\n\t"
      "s_waitcnt lgkmcnt(8)\n\t"
      "v_pk_add_f32 v[0:1], v[0:1], v[64:65]\n\t"
      "v_pk_add_f32 v[2:3], v[2:3], v[66:67]\n\t"
      "v_pk_add_f32 v[4:5], v[4:5], v[68:69]\n\t"
      "v_pk_add_f32 v[6:7], v[6:7], v[70:71]\n\t"
      "v_pk_add_f32 v[8:9], v[8:9], v[72:73]\n\t"
      "v_pk_add_f32 v[10:11], v[10:11], v[74:75]\n\t"
      "v_pk_add_f32 v[12:13], v[12:13], v[76:77]\n\t"
      "v_pk_add_f32 v[14:15], v[14:15], v[78:79]\n\t"
      "s_waitcnt lgkmcnt(4)\n\t"
      "v_pk_add_f32 v[0:1], v[0:1], v[80:81]\n\t"
      "v_pk_add_f32 v[2:3], v[2:3], v[82:83]\n\t"
      "v_pk_add_f32 v[4:5], v[4:5], v[84:85]\n\t"
      "v_pk_add_f32 v[6:7], v[6:7], v[86:87]\n\t"
      "v_pk_add_f32 v[8:9], v[8:9], v[88:89]\n\t"
      "v_pk_add_f32 v[10:11], v[10:11], v[90:91]\n\t"
      "v_pk_add_f32 v[12:13], v[12:13], v[92:93]\n\t"
      "v_pk_add_f32 v[14:15], v[14:15], v[94:95]\n\t"
      "s_waitcnt lgkmcnt(0)\n\t"
      "v_pk_add_f32 v[0:1], v[0:1], v[96:97]\n\t"
      "v_pk_add_f32 v[2:3], v[2:3], v[98:99]\n\t"
      "v_pk_add_f32 v[4:5], v[4:5], v[100:101]\n\t"
      "v_pk_add_f32 v[6:7], v[6:7], v[102:103]\n\t"
      "v_pk_add_f32 v[8:9], v[8:9], v[104:105]\n\t"
      "v_pk_add_f32 v[10:11], v[10:11], v[106:107]\n\t"
      "v_pk_add_f32 v[12:13], v[12:13], v[108:109]\n\t"
      "v_pk_add_f32 v[14:15], v[14:15], v[110:111]\n\t"
      "v_add_f32 v0, v0, v148\n\t"
      "v_add_f32 v1, v1, v148\n\t"
      "v_add_f32 v2, v2, v148\n\t"
      "v_add_f32 v3, v3, v148\n\t"
      "v_add_f32 v4, v4, v148\n\t"
      "v_add_f32 v5, v5, v148\n\t"
      "v_add_f32 v6, v6, v148\n\t"
      "v_add_f32 v7, v7, v148\n\t"
      "v_add_f32 v8, v8, v148\n\t"
      "v_add_f32 v9, v9, v148\n\t"
      "v_add_f32 v10, v10, v148\n\t"
      "v_add_f32 v11, v11, v148\n\t"
      "v_add_f32 v12, v12, v148\n\t"
      "v_add_f32 v13, v13, v148\n\t"
      "v_add_f32 v14, v14, v148\n\t"
      "v_add_f32 v15, v15, v148\n\t"
      "v_cvt_pk_bf16_f32 v32, v0, v1\n\t"
      "v_cvt_pk_bf16_f32 v33, v2, v3\n\t"
      "v_cvt_pk_bf16_f32 v34, v4, v5\n\t"
      "v_cvt_pk_bf16_f32 v35, v6, v7\n\t"
      "v_cvt_pk_bf16_f32 v36, v8, v9\n\t"
      "v_cvt_pk_bf16_f32 v37, v10, v11\n\t"
      "v_cvt_pk_bf16_f32 v38, v12, v13\n\t"
      "v_cvt_pk_bf16_f32 v39, v14, v15\n\t"
      "ds_write_b128 v202, v[32:35]\n\t"
      "ds_write_b128 v202, v[36:39] offset:16\n\t"
      "s_waitcnt lgkmcnt(0)\n\t"
      "s_barrier\n\t"
      "ds_read_b128 v[32:35], v203\n\t"
      "ds_read_b128 v[36:39], v203 offset:1152\n\t"
      "s_waitcnt lgkmcnt(1)\n\t"
      "buffer_store_dwordx4 v[32:35], v155, s[52:55], s95 offen sc1\n\t"
      "s_add_u32 s95, s95, 8192\n\t"
      "s_waitcnt lgkmcnt(0)\n\t"
      "buffer_store_dwordx4 v[36:39], v155, s[52:55], s95 offen sc1\n\t"
      "kend%=:\n\t"
      :
      : [ka] "s"(ka), [bid] "s"(blockIdx.x), [tid] "v"(threadIdx.x), [ldsb] "s"(ldsb)
      : "v0", "v1", "v2", "v3", "v4", "v5", "v6", "v7", "v8", "v9", "v10", "v11", "v12", "v13", "v14", "v15", "v16", "v17", "v18", "v19", "v20", "v21", "v22", "v23", "v24", "v25", "v26", "v27", "v28", "v29", "v30", "v31", "v32", "v33", "v34", "v35", "v36", "v37", "v38", "v39", "v40", "v41", "v42", "v43", "v44", "v45", "v46", "v47", "v48", "v49", "v50", "v51", "v52", "v53", "v54", "v55", "v56", "v57", "v58", "v59", "v60", "v61", "v62", "v63", "v64", "v65", "v66", "v67", "v68", "v69", "v70", "v71", "v72", "v73", "v74", "v75", "v76", "v77", "v78", "v79", "v80", "v81", "v82", "v83", "v84", "v85", "v86", "v87", "v88", "v89", "v90", "v91", "v92", "v93", "v94", "v95", "v96", "v97", "v98", "v99", "v100", "v101", "v102", "v103", "v104", "v105", "v106", "v107", "v108", "v109", "v110", "v111", "v112", "v113", "v114", "v115", "v116", "v117", "v118", "v119", "v120", "v121", "v122", "v123", "v124", "v125", "v126", "v127", "v128", "v129", "v130", "v131", "v132", "v133", "v134", "v135", "v136", "v137", "v138", "v139", "v140", "v141", "v142", "v143", "v144", "v145", "v146", "v147", "v148", "v149", "v150", "v151", "v152", "v153", "v154", "v155", "v156", "v157", "v158", "v159", "v160", "v161", "v162", "v163", "v164", "v165", "v166", "v167", "v168", "v169", "v170", "v171", "v172", "v173", "v174", "v175", "v176", "v177", "v178", "v179", "v180", "v181", "v182", "v183", "v184", "v185", "v186", "v187", "v188", "v189", "v190", "v191", "v192", "v193", "v194", "v195", "v196", "v197", "v198", "v199", "v200", "v201", "v202", "v203", "v204", "v205", "v206", "v207", "v208", "v209", "v210", "v211", "v212", "v213", "a0", "a1", "a2", "a3", "a4", "a5", "a6", "a7", "a8", "a9", "a10", "a11", "a12", "a13", "a14", "a15", "a16", "a17", "a18", "a19", "a20", "a21", "a22", "a23", "a24", "a25", "a26", "a27", "a28", "a29", "a30", "a31", "a32", "a33", "a34", "a35", "a36", "a37", "a38", "a39", "a40", "a41", "a42", "a43", "a44", "a45", "a46", "a47", "a48", "a49", "a50", "a51", "a52", "a53", "a54", "a55", "a56", "a57", "a58", "a59", "a60", "a61", "a62", "a63", "a64", "a65", "a66", "a67", "a68", "a69", "a70", "a71", "a72", "a73", "a74", "a75", "a76", "a77", "a78", "a79", "a80", "a81", "a82", "a83", "a84", "a85", "a86", "a87", "a88", "a89", "a90", "a91", "a92", "a93", "a94", "a95", "a96", "a97", "a98", "a99", "a100", "a101", "a102", "a103", "a104", "a105", "a106", "a107", "a108", "a109", "a110", "a111", "a112", "a113", "a114", "a115", "a116", "a117", "a118", "a119", "a120", "a121", "a122", "a123", "a124", "a125", "a126", "a127", "a128", "a129", "a130", "a131", "a132", "a133", "a134", "a135", "a136", "a137", "a138", "a139", "a140", "a141", "a142", "a143", "a144", "a145", "a146", "a147", "a148", "a149", "a150", "a151", "a152", "a153", "a154", "a155", "a156", "a157", "a158", "a159", "a160", "a161", "a162", "a163", "a164", "a165", "a166", "a167", "a168", "a169", "a170", "a171", "a172", "a173", "a174", "a175", "a176", "a177", "a178", "a179", "a180", "a181", "a182", "a183", "a184", "a185", "a186", "a187", "a188", "a189", "a190", "a191", "a192", "a193", "a194", "a195", "a196", "a197", "a198", "a199", "a200", "a201", "a202", "a203", "a204", "a205", "a206", "a207", "a208", "a209", "a210", "a211", "a212", "a213", "a214", "a215", "a216", "a217", "a218", "a219", "a220", "a221", "a222", "a223", "a224", "a225", "a226", "a227", "a228", "a229", "a230", "a231", "a232", "a233", "a234", "a235", "a236", "a237", "a238", "a239", "a240", "a241", "a242", "a243", "a244", "a245", "a246", "a247", "a248", "a249", "a250", "a251", "a252", "a253", "a254", "a255", "s4", "s5", "s6", "s7", "s8", "s9", "s10", "s11", "s12", "s13", "s14", "s15", "s16", "s17", "s18", "s19", "s20", "s21", "s22", "s23", "s24", "s25", "s26", "s27", "s28", "s29", "s30", "s31", "s34", "s35", "s36", "s37", "s38", "s39", "s40", "s41", "s42", "s43", "s44", "s45", "s46", "s47", "s48", "s49", "s50", "s51", "s52", "s53", "s54", "s55", "s56", "s57", "s58", "s59", "s60", "s61", "s62", "s63", "s64", "s65", "s66", "s67", "s68", "s69", "s70", "s71", "s72", "s73", "s74", "s75", "s76", "s77", "s78", "s79", "s80", "s81", "s82", "s83", "s84", "s85", "s86", "s87", "s88", "s89", "s90", "s91", "s92", "s93", "s94", "s95", "s96", "s97", "s98", "s99", "vcc", "scc", "memory");
}


extern "C" void kernel_launch(void* const* d_in, const int* in_sizes, int n_in,
                              void* d_out, int out_size, void* d_ws, size_t ws_size,
                              hipStream_t stream) {
  (void)hipFuncSetAttribute((const void*)mixer_token_mlp, hipFuncAttributeMaxDynamicSharedMemorySize, LDS_BYTES);
  mixer_token_mlp<<<256, 256, LDS_BYTES, stream>>>(d_in[0], d_in[1], d_in[2], d_in[3], d_in[4], d_out, (char*)d_ws + (0));
}
```
